```python
import torch

def _softmax(x, axis=-1):
    return torch.softmax(x, dim=axis)
def _gelu(x):
    return torch.nn.functional.gelu(x, approximate="tanh")
def _silu(x):
    return torch.nn.functional.silu(x)
def _sigmoid(x):
    return torch.sigmoid(x)
def _layernorm(x, axis=-1, eps=1e-5):
    m = x.mean(dim=axis, keepdim=True)
    v = x.var(dim=axis, keepdim=True, unbiased=False)
    return (x - m) / torch.sqrt(v + eps)
def _scatter_add(tgt, idx, src, axis=0):
    return tgt.clone().index_add_(axis, idx, src)
def _segment_sum(src, seg, n):
    out = torch.zeros((n,) + src.shape[1:], dtype=src.dtype)
    return out.index_add_(0, seg, src)
def _pad(x, pw):
    flat = tuple(v for pair in reversed(pw) for v in pair)
    return torch.nn.functional.pad(x, flat)

kernel_name = 'recurrent_scan__sliding_block_cached__swiglu_ffn__decode'

def setup_inputs():

    _w = (lambda a: (lambda t: t.to(torch.bfloat16) if t.is_floating_point() else t)(torch.as_tensor(a)))
    def _A_setup():
        B, Sq, D = 2, 1, 512
        Hq, Hkv, Sk, Dh = 32, 32, 8192, 128
        assert Hq % Hkv == 0, (Hq, Hkv)
        return {
            "x": _w((lambda *s: torch.randn(*s, dtype=torch.bfloat16) * 0.05)(B, Sq, D)),
            "k_cache": _w((lambda *s: torch.randn(*s, dtype=torch.bfloat16) * 1.6)(B, Hkv, Sk, Dh)),
            "v_cache": _w((lambda *s: torch.randn(*s, dtype=torch.bfloat16) * 0.2)(B, Hkv, Sk, Dh)),
            "wq": _w((lambda *s: torch.randn(*s, dtype=torch.bfloat16) * 1.6)(D, Hq * Dh) * (D ** -0.5)),
            "wo": _w((lambda *s: torch.randn(*s, dtype=torch.bfloat16) * 0.2)(Hq * Dh, D) * ((Hq * Dh) ** -0.5)),
        }
    _A_in = _A_setup()

    def _B_setup():
        B, S, D, F = 2, 1, 512, 1376
        return {
            "x": _w((lambda *s: torch.randn(*s, dtype=torch.bfloat16) * 0.05)(B, S, D)),
            "w_gate": _w((lambda *s: torch.randn(*s, dtype=torch.bfloat16) * 0.1)(D, F)),
            "w_up": _w((lambda *s: torch.randn(*s, dtype=torch.bfloat16) * 0.1)(D, F)),
            "w_down": _w((lambda *s: torch.randn(*s, dtype=torch.bfloat16) * 0.1)(F, D)),
        }
    _B_in = _B_setup()

    return {
        "A_x": _A_in["x"],
        "A_k_cache": _A_in["k_cache"],
        "A_v_cache": _A_in["v_cache"],
        "A_wq": _A_in["wq"],
        "A_wo": _A_in["wo"],
        "B_x": _B_in["x"],
        "B_w_gate": _B_in["w_gate"],
        "B_w_up": _B_in["w_up"],
        "B_w_down": _B_in["w_down"],
    }

def reference(A_x, A_k_cache, A_v_cache, A_wq, A_wo, B_x, B_w_gate, B_w_up, B_w_down):
    def _A_ref(x, k_cache, v_cache, wq, wo):
        B, Sq, D = x.shape
        Hq, Dh = 32, 128
        Hkv, Sk = k_cache.shape[1], k_cache.shape[2]
        W = min(4096, max(256, Sk // 4))
        rep = Hq // Hkv
        q = (lambda x, *d: x.permute(*d))((x @ wq).reshape(B, Sq, Hq, Dh), 0, 2, 1, 3)
        k = (lambda x, n, axis: torch.repeat_interleave(x, n, dim=axis))(k_cache[:, :, Sk - W:, :], rep, axis=1)
        v = (lambda x, n, axis: torch.repeat_interleave(x, n, dim=axis))(v_cache[:, :, Sk - W:, :], rep, axis=1)
        scores = torch.einsum("bhqd,bhkd->bhqk", q, k) * (Dh ** -0.5)
        p = _softmax(scores, axis=-1)
        o = torch.einsum("bhqk,bhkd->bhqd", p, v)
        return (lambda x, *d: x.permute(*d))(o, 0, 2, 1, 3).reshape(B, Sq, Hq * Dh) @ wo

    def _B_ref(x, w_gate, w_up, w_down):
        g = _silu(x @ w_gate)
        u = x @ w_up
        return (g * u) @ w_down

    x = A_x
    xn = _layernorm(x, axis=-1)
    s = _A_ref(xn, A_k_cache, A_v_cache, A_wq, A_wo)
    g = _silu(_B_ref(xn, B_w_gate, B_w_up, B_w_down))
    return x + s * g
```

```cpp
#include <hip/hip_runtime.h>
#include <hip/hip_bf16.h>
#include <stdint.h>

typedef unsigned short u16;
__device__ __forceinline__ float bf2f(u16 b){ return __uint_as_float(((unsigned)b)<<16); }
__device__ __forceinline__ u16 f2bf(float f){ unsigned u=__float_as_uint(f); u += 0x7FFFu + ((u>>16)&1u); return (u16)(u>>16); }
__device__ __forceinline__ float rbf(float f){ return bf2f(f2bf(f)); }

constexpr int B=2, D=512, H=32, DH=128, SK=8192, W=2048, F=1376;

__device__ void layernorm_to_lds(const u16* x, float* xn  , float* red) {
  int tid = threadIdx.x;
  if (tid < 128) {
    int b = tid >> 6, l = tid & 63;
    float v[8]; float s=0;
    for (int i=0;i<8;i++){ v[i]=bf2f(x[b*D + l*8 + i]); s+=v[i]; }
    for (int o=32;o;o>>=1) s += __shfl_xor(s,o);
    float m = s*(1.f/D);
    float q=0; for (int i=0;i<8;i++){ float d=v[i]-m; q+=d*d; }
    for (int o=32;o;o>>=1) q += __shfl_xor(q,o);
    float var = q*(1.f/D);
    float mb = rbf(m), vb = rbf(var);
    float den = rbf(sqrtf(rbf(vb + 1e-5f)));
    for (int i=0;i<8;i++) xn[b*D + l*8+i] = rbf(rbf(v[i]-mb)/den);
  }
  __syncthreads();
}

__global__ void k1(const u16* x, const u16* wq, const u16* wg, const u16* wu, float* q, float* hid) {
  __shared__ float xn[2*D]; __shared__ float red[4];
  layernorm_to_lds(x, xn, red);
  int c = blockIdx.x*blockDim.x + threadIdx.x;
  if (c < H*DH) {
    float a0=0,a1=0;
    for (int k=0;k<D;k++){ float w=bf2f(wq[k*(H*DH)+c]); a0+=xn[k]*w; a1+=xn[D+k]*w; }
    q[c]=rbf(a0); q[H*DH+c]=rbf(a1);
  } else if (c < H*DH+F) {
    int j=c-H*DH; float g0=0,g1=0,u0=0,u1=0;
    for (int k=0;k<D;k++){ float w=bf2f(wg[k*F+j]), w2=bf2f(wu[k*F+j]); g0+=xn[k]*w; g1+=xn[D+k]*w; u0+=xn[k]*w2; u1+=xn[D+k]*w2; }
    g0=rbf(g0); g1=rbf(g1); u0=rbf(u0); u1=rbf(u1);
    float s0=rbf(g0/(1.f+__expf(-g0))), s1=rbf(g1/(1.f+__expf(-g1)));
    hid[j]=rbf(s0*u0); hid[F+j]=rbf(s1*u1);
  }
}
__global__ void k2(const u16* kc, const u16* vc, const float* q, float* o) {
  __shared__ float sc[W]; __shared__ float qs[DH]; __shared__ float red[16]; __shared__ float oacc[8][DH];
  int bh=blockIdx.x, tid=threadIdx.x; int b=bh/H,h=bh%H;
  if (tid<DH) qs[tid]=q[b*H*DH+h*DH+tid];
  __syncthreads();
  const u16* K = kc + ((size_t)bh*SK + (SK-W))*DH;
  const u16* V = vc + ((size_t)bh*SK + (SK-W))*DH;
  float mx=-1e30f;
  for (int j=tid;j<W;j+=blockDim.x){ float s=0; for(int d=0;d<DH;d++) s+=qs[d]*bf2f(K[(size_t)j*DH+d]); s*=0.08838834764831845f; sc[j]=s; mx=fmaxf(mx,s);}
  for (int o2=32;o2;o2>>=1) mx=fmaxf(mx,__shfl_xor(mx,o2));
  if((tid&63)==0) red[tid>>6]=mx; __syncthreads();
  mx=red[0]; for(int i=1;i<(int)blockDim.x/64;i++) mx=fmaxf(mx,red[i]); __syncthreads();
  float sum=0; for (int j=tid;j<W;j+=blockDim.x){ float p=__expf(sc[j]-mx); sc[j]=p; sum+=p; }
  for (int o2=32;o2;o2>>=1) sum+=__shfl_xor(sum,o2);
  if((tid&63)==0) red[tid>>6]=sum; __syncthreads();
  sum=0; for(int i=0;i<(int)blockDim.x/64;i++) sum+=red[i];
  int d=tid&127, g=tid>>7; float a=0;
  for (int j=g;j<W;j+=8) a+=sc[j]*bf2f(V[(size_t)j*DH+d]);
  oacc[g][d]=a; __syncthreads();
  if (tid<DH){ float t=0; for(int i=0;i<8;i++) t+=oacc[i][tid]; o[b*H*DH+h*DH+tid]=rbf(t/sum); }
}
__global__ void k3(const u16* x, const u16* wo, const u16* wd, const float* o, const float* hid, u16* out) {
  int c = blockIdx.x*blockDim.x+threadIdx.x;
  int b=c/D, n=c%D;
  float s=0; for(int k=0;k<H*DH;k++) s+=o[b*H*DH+k]*bf2f(wo[k*D+n]);
  float d=0; for(int k=0;k<F;k++) d+=hid[b*F+k]*bf2f(wd[k*D+n]);
  s=rbf(s); d=rbf(d); float g=rbf(d/(1.f+__expf(-d)));
  out[c]=f2bf(bf2f(x[c])+rbf(s*g));
}

extern "C" void kernel_launch(void* const* d_in, const int* in_sizes, int n_in, void* d_out, int out_size, void* d_ws, size_t ws_size, hipStream_t stream) {
  const u16* x=(const u16*)d_in[0]; const u16* kc=(const u16*)d_in[1]; const u16* vc=(const u16*)d_in[2];
  const u16* wq=(const u16*)d_in[3]; const u16* wo=(const u16*)d_in[4];
  const u16* wg=(const u16*)d_in[6]; const u16* wu=(const u16*)d_in[7]; const u16* wd=(const u16*)d_in[8];
  float* q=(float*)d_ws; float* hid=q+2*4096; float* o=hid+2*1376+32;
  k1<<<(4096+1376+127)/128,128,0,stream>>>(x,wq,wg,wu,q,hid);
  k2<<<64,1024,0,stream>>>(kc,vc,q,o);
  k3<<<16,64,0,stream>>>(x,wo,wd,o,hid,(u16*)d_out);
}
```

```cpp
#include <hip/hip_runtime.h>
#include <stdint.h>

typedef unsigned u32;
typedef unsigned long long u64;
typedef u32 v4u __attribute__((ext_vector_type(4)));
typedef u32 v2u __attribute__((ext_vector_type(2)));
typedef float v2f __attribute__((ext_vector_type(2)));
typedef float v4f __attribute__((ext_vector_type(4)));
typedef __bf16 bf2 __attribute__((ext_vector_type(2)));
#define LAS __attribute__((address_space(3)))

#define STAMP 0

constexpr int SK = 8192, WIN = 2048;
constexpr int AUX_NT = 2, AUX_SC1 = 16;
constexpr int WS_EPOCH = 0;
constexpr int WS_X0 = 4096;
constexpr int WS_X1 = WS_X0 + 256 * 4096;
constexpr int X1_STRIDE = 1152;
constexpr int WS_X2 = WS_X1 + 256 * X1_STRIDE;
constexpr int WS_END = WS_X2 + 256 * 2048;
constexpr int WS_STAMP = 4 << 20;
constexpr u32 SPIN_CAP = 1u << 22;

__device__ __forceinline__ float dot2(u32 a, u32 b, float c) {
  return __builtin_amdgcn_fdot2_f32_bf16(__builtin_bit_cast(bf2, a), __builtin_bit_cast(bf2, b), c, false);
}
__device__ __forceinline__ u32 cvtpk(float lo, float hi) {
  bf2 v = {(__bf16)lo, (__bf16)hi};
  return __builtin_bit_cast(u32, v);
}
__device__ __forceinline__ float rbf(float x) { return (float)(__bf16)x; }
__device__ __forceinline__ float bflo(u32 v) { return __builtin_bit_cast(float, v << 16); }
__device__ __forceinline__ float bfhi(u32 v) { return __builtin_bit_cast(float, v & 0xffff0000u); }
__device__ __forceinline__ u32 plo(u32 a, u32 b) { return __builtin_amdgcn_perm(b, a, 0x05040100u); }
__device__ __forceinline__ u32 phi(u32 a, u32 b) { return __builtin_amdgcn_perm(b, a, 0x07060302u); }
template <int CTRL> __device__ __forceinline__ float dppf(float x) {
  return __builtin_bit_cast(float, __builtin_amdgcn_mov_dpp(__builtin_bit_cast(int, x), CTRL, 0xf, 0xf, true));
}
template <int CTRL> __device__ __forceinline__ u32 dppu(u32 x) {
  return (u32)__builtin_amdgcn_mov_dpp((int)x, CTRL, 0xf, 0xf, true);
}
constexpr int XOR1 = 0xB1, XOR2 = 0x4E, XOR7 = 0x141, XOR8 = 0x128, ROR4 = 0x124;
__device__ __forceinline__ float red32(float a, float b) {
  auto s = __builtin_amdgcn_permlane32_swap(__builtin_bit_cast(u32, a), __builtin_bit_cast(u32, b), false, false);
  return __builtin_bit_cast(float, (u32)s[0]) + __builtin_bit_cast(float, (u32)s[1]);
}
__device__ __forceinline__ float red16(float a, float b) {
  auto s = __builtin_amdgcn_permlane16_swap(__builtin_bit_cast(u32, a), __builtin_bit_cast(u32, b), false, false);
  return __builtin_bit_cast(float, (u32)s[0]) + __builtin_bit_cast(float, (u32)s[1]);
}
__device__ __forceinline__ float wave_sum(float x) {
  x += dppf<XOR1>(x); x += dppf<XOR2>(x); x += dppf<XOR7>(x); x += dppf<XOR8>(x);
  x = red16(x, x); x = red32(x, x);
  return x;
}
__device__ __forceinline__ float wave_max(float x) {
  x = fmaxf(x, dppf<XOR1>(x)); x = fmaxf(x, dppf<XOR2>(x)); x = fmaxf(x, dppf<XOR7>(x)); x = fmaxf(x, dppf<XOR8>(x));
  auto s = __builtin_amdgcn_permlane16_swap(__builtin_bit_cast(u32, x), __builtin_bit_cast(u32, x), false, false);
  x = fmaxf(__builtin_bit_cast(float, (u32)s[0]), __builtin_bit_cast(float, (u32)s[1]));
  auto t = __builtin_amdgcn_permlane32_swap(__builtin_bit_cast(u32, x), __builtin_bit_cast(u32, x), false, false);
  return fmaxf(__builtin_bit_cast(float, (u32)t[0]), __builtin_bit_cast(float, (u32)t[1]));
}
#define SB() __builtin_amdgcn_sched_barrier(0)
#define WGBAR() do { SB(); asm volatile("s_waitcnt lgkmcnt(0)" ::: "memory"); __builtin_amdgcn_s_barrier(); asm volatile("" ::: "memory"); SB(); } while (0)
#define RSRC(p, n) __builtin_amdgcn_make_buffer_rsrc((void*)(p), 0, (n), 0x00020000)
#define LD16(rs, voff, aux) __builtin_amdgcn_raw_buffer_load_b128((rs), (voff), 0, (aux))
#define LD16S(rs, voff, soff, aux) __builtin_amdgcn_raw_buffer_load_b128((rs), (voff), (soff), (aux))
#define ST16(v, rs, voff) __builtin_amdgcn_raw_buffer_store_b128((v), (rs), (voff), 0, AUX_SC1)
#define ST8(v, rs, voff) __builtin_amdgcn_raw_buffer_store_b64((v), (rs), (voff), 0, AUX_SC1)
__device__ __forceinline__ u32 fbits(float f) { return __builtin_bit_cast(u32, f); }
__device__ __forceinline__ float bitsf(u32 u) { return __builtin_bit_cast(float, u); }
__device__ __forceinline__ float silu_f(float x) { return x * __builtin_amdgcn_rcpf(1.0f + __builtin_amdgcn_exp2f(-1.4426950408889634f * x)); }

#define STAMP_AT(k) do {} while (0)

template <int NP, int NB>
__device__ __forceinline__ void tile_gemv(const v4u (&T)[2 * NP], const u32 (&cf)[NB][NP], float (&acc)[NB][8]) {
#pragma unroll
  for (int k = 0; k < NP; ++k) {
#pragma unroll
    for (int e = 0; e < 4; ++e) {
      const u32 lo = plo(T[2 * k][e], T[2 * k + 1][e]), hi = phi(T[2 * k][e], T[2 * k + 1][e]);
#pragma unroll
      for (int b = 0; b < NB; ++b) {
        acc[b][2 * e] = dot2(lo, cf[b][k], acc[b][2 * e]);
        acc[b][2 * e + 1] = dot2(hi, cf[b][k], acc[b][2 * e + 1]);
      }
    }
  }
}

__global__ void __launch_bounds__(512, 2)
fused_decode(const void* __restrict__ xp, const void* __restrict__ kc, const void* __restrict__ vc,
             const void* __restrict__ wq, const void* __restrict__ wo, const void* __restrict__ wg,
             const void* __restrict__ wu, const void* __restrict__ wd, void* __restrict__ outp, void* __restrict__ ws) {
  __shared__ __attribute__((aligned(16))) u32 lds[64 + 256 + 64 + 128 + 16 + 512];
  constexpr int L_Q = 0, L_GU = 64, L_HB = 320, L_OB = 384, L_ML = 512, L_RED = 528;
  const int c = blockIdx.x;
  const int h = (c & 7) * 4 + (c >> 6), j = (c >> 3) & 7, b = j >> 2, s = j & 3;
  const int id = h * 8 + j;
  const int lane = threadIdx.x & 63, w = __builtin_amdgcn_readfirstlane(threadIdx.x >> 6);
  STAMP_AT(0);

  if (w >= 4) {
    const int sw = w - 4;
    const size_t kvoff = (((size_t)(b * 32 + h)) * SK + (SK - WIN) + s * 512) * 256;
    const auto rk = RSRC((const char*)kc + kvoff, 131072);
    const auto rv = RSRC((const char*)vc + kvoff, 131072);
    const int lin = sw * 1024 + lane * 16;
    constexpr int NV0 = 12;
    v4u K[32], V[32];
#pragma unroll
    for (int i = 0; i < 32; ++i) K[i] = LD16S(rk, lin, i * 4096, AUX_NT);
    SB();
#pragma unroll
    for (int i = 0; i < NV0; ++i) V[i] = LD16S(rv, lin, i * 4096, AUX_NT);
    STAMP_AT(1);
    WGBAR();
    STAMP_AT(2);
    const int cc = lane & 15;
    const v4u q = *(const LAS v4u*)&lds[L_Q + cc * 4];
    float sc[2];
#pragma unroll
    for (int g = 0; g < 2; ++g) {
      float x[16];
#pragma unroll
      for (int i = 0; i < 16; ++i) {
        const v4u kk = K[2 * i + g];
        float d = dot2(kk[0], q[0], 0.f);
        d = dot2(kk[1], q[1], d); d = dot2(kk[2], q[2], d); d = dot2(kk[3], q[3], d);
        x[i] = d;
      }
#define TR_STEP(HALF, CTRL) _Pragma("unroll") for (int t = 0; t < (HALF); ++t) { \
        const float t1 = x[t] + dppf<CTRL>(x[t]), t2 = x[t + (HALF)] + dppf<CTRL>(x[t + (HALF)]); x[t] = (cc & (HALF)) ? t2 : t1; }
      TR_STEP(8, XOR8) TR_STEP(4, XOR7) TR_STEP(2, XOR2) TR_STEP(1, XOR1)
#undef TR_STEP
      sc[g] = rbf(rbf(x[0]) * 0.08838834764831845f);
    }
    SB();
#pragma unroll
    for (int i = NV0; i < NV0 + 8; ++i) V[i] = LD16S(rv, lin, i * 4096, AUX_NT);
    SB();
    const float m = wave_max(fmaxf(sc[0], sc[1]));
    const float p0 = __builtin_amdgcn_exp2f((sc[0] - m) * 1.4426950408889634f);
    const float p1 = __builtin_amdgcn_exp2f((sc[1] - m) * 1.4426950408889634f);
    const float l = wave_sum(p0 + p1);
    const u32 pp = cvtpk(p0, p1);
    if (lane == 0) *(LAS v2f*)&lds[L_ML + sw * 2] = v2f{m, l};
    float acc[8];
#pragma unroll
    for (int e = 0; e < 8; ++e) acc[e] = 0.f;
    STAMP_AT(3);
#define PV_STEP(i) do { const u32 pb = dppu<0x150 + (i)>(pp); \
      _Pragma("unroll") for (int e = 0; e < 4; ++e) { \
        acc[2 * e] = dot2(plo(V[2 * (i)][e], V[2 * (i) + 1][e]), pb, acc[2 * e]); \
        acc[2 * e + 1] = dot2(phi(V[2 * (i)][e], V[2 * (i) + 1][e]), pb, acc[2 * e + 1]); } } while (0)
#define V_ISSUE(i) do { V[i] = LD16S(rv, lin, (i) * 4096, AUX_NT); V[(i) + 1] = LD16S(rv, lin, ((i) + 1) * 4096, AUX_NT); SB(); } while (0)
    PV_STEP(0); SB(); V_ISSUE(20);
    PV_STEP(1); SB(); V_ISSUE(22);
    PV_STEP(2); SB(); V_ISSUE(24);
    PV_STEP(3); SB(); V_ISSUE(26);
    PV_STEP(4); SB(); V_ISSUE(28);
    PV_STEP(5); SB(); V_ISSUE(30);
    STAMP_AT(4);
    WGBAR();
    PV_STEP(6); SB(); PV_STEP(7); SB(); PV_STEP(8); SB(); PV_STEP(9); SB(); PV_STEP(10); SB();
    PV_STEP(11); SB(); PV_STEP(12); SB(); PV_STEP(13); SB(); PV_STEP(14); SB(); PV_STEP(15); SB();
    STAMP_AT(5);
    float t[4];
#pragma unroll
    for (int e = 0; e < 4; ++e) t[e] = red32(acc[e], acc[e + 4]);
    const float u0 = red16(t[0], t[2]), u1 = red16(t[1], t[3]);
    const int R = lane >> 4;
    *(LAS v2f*)&lds[L_RED + sw * 128 + cc * 8 + (R >> 1) * 4 + (R & 1) * 2] = v2f{u0, u1};
    WGBAR();
    STAMP_AT(6);
    return;
  }

  const auto rws = RSRC(ws, WS_END);
  const int r8 = lane >> 3, c8 = lane & 7;
  const int ch0 = (172 * h) >> 5, nch = ((172 * (h + 1)) >> 5) - ch0;
  u32 tag;
  {
    const auto rx = RSRC(xp, 2048);
    v4u xa[2], xr[2];
#pragma unroll
    for (int bb = 0; bb < 2; ++bb) xa[bb] = LD16(rx, bb * 1024 + lane * 16, 0);
#pragma unroll
    for (int bb = 0; bb < 2; ++bb) xr[bb] = LD16(rx, bb * 1024 + (8 * j + r8) * 16, 0);
    const char* base; int rstride, nbytes, colb;
    if (w < 2) { base = (const char*)wq; rstride = 8192; nbytes = 512 * 8192; colb = h * 256 + w * 128; }
    else { base = (const char*)(w == 2 ? wg : wu); rstride = 2752; nbytes = 512 * 2752; colb = ch0 * 16; }
    const auto rt = RSRC(base, nbytes);
    const bool act = (w < 2) || (c8 < nch);
    const int toff = act ? ((64 * j + 8 * r8) * rstride + colb + c8 * 16) : 0x7ff00000;
    v4u T[8];
#pragma unroll
    for (int i = 0; i < 8; ++i) T[i] = LD16(rt, toff + i * rstride, AUX_NT);
    const u32 ep = __builtin_amdgcn_raw_buffer_load_b32(rws, WS_EPOCH, 0, AUX_SC1);
    SB();
    u32 cf[2][4];
#pragma unroll
    for (int bb = 0; bb < 2; ++bb) {
      float v[8];
#pragma unroll
      for (int e = 0; e < 4; ++e) { v[2 * e] = bflo(xa[bb][e]); v[2 * e + 1] = bfhi(xa[bb][e]); }
      float sum = ((v[0] + v[1]) + (v[2] + v[3])) + ((v[4] + v[5]) + (v[6] + v[7]));
      const float mean = wave_sum(sum) * (1.0f / 512.0f);
      float sq = 0.f;
#pragma unroll
      for (int e = 0; e < 8; ++e) { const float d = v[e] - mean; sq = fmaf(d, d, sq); }
      const float var = wave_sum(sq) * (1.0f / 512.0f);
      const float mb = rbf(mean);
      const float den = rbf(__builtin_sqrtf(rbf(rbf(var) + 1e-5f)));
      const float inv = __builtin_amdgcn_rcpf(den);
#pragma unroll
      for (int e = 0; e < 4; ++e)
        cf[bb][e] = cvtpk(rbf(bflo(xr[bb][e]) - mb) * inv, rbf(bfhi(xr[bb][e]) - mb) * inv);
    }
    float acc[2][8];
#pragma unroll
    for (int bb = 0; bb < 2; ++bb)
#pragma unroll
      for (int e = 0; e < 8; ++e) acc[bb][e] = 0.f;
    tile_gemv<4, 2>(T, cf, acc);
    tag = __builtin_amdgcn_readfirstlane(ep) + 1u;
    float t[8];
#pragma unroll
    for (int e = 0; e < 8; ++e) t[e] = red32(acc[0][e], acc[1][e]);
    float u[4];
#pragma unroll
    for (int e = 0; e < 4; ++e) u[e] = red16(t[e], t[e + 4]);
    const bool hi8 = (lane & 8) != 0;
    const float a0 = u[0] + dppf<XOR8>(u[0]), a1 = u[1] + dppf<XOR8>(u[1]);
    const float a2 = u[2] + dppf<XOR8>(u[2]), a3 = u[3] + dppf<XOR8>(u[3]);
    const float r0 = hi8 ? a2 : a0, r1 = hi8 ? a3 : a1;
    const int pos = (lane >> 5) * 64 + c8 * 8 + ((lane >> 4) & 1) * 4 + ((lane >> 3) & 1) * 2;
    STAMP_AT(1);
    if (act) ST16((v4u{fbits(r0), tag, fbits(r1), tag}), rws, WS_X0 + id * 4096 + w * 1024 + pos * 8);
  }
  {
    const bool need = (w < 2) ? ((lane >> 5) == b) : ((lane & 31) < nch * 4);
    const int goff = WS_X0 + h * 8 * 4096 + w * 1024 + lane * 16;
    float s0 = 0.f, s1 = 0.f;
    __builtin_amdgcn_s_sleep(20);
    for (u32 spin = 0; spin < SPIN_CAP; ++spin) {
      u32 bad = 0;
      s0 = 0.f; s1 = 0.f;
      asm volatile("" ::: "memory");
      if (need) {
        v4u g[8];
#pragma unroll
        for (int i = 0; i < 8; ++i) g[i] = LD16(rws, goff + i * 4096, AUX_SC1);
#pragma unroll
        for (int i = 0; i < 8; ++i) { bad |= (g[i][1] ^ tag) | (g[i][3] ^ tag); s0 += bitsf(g[i][0]); s1 += bitsf(g[i][2]); }
      }
      if (__all(bad == 0)) break;
      __builtin_amdgcn_s_sleep(4);
    }
    if (w < 2) { if (need) lds[L_Q + w * 32 + (lane & 31)] = cvtpk(s0, s1); }
    else *(LAS v2f*)&lds[L_GU + (w - 2) * 128 + lane * 2] = v2f{s0, s1};
  }
  STAMP_AT(2);
  WGBAR();
  u32 xres = 0;
  if (w == 2) {
    const v2f gg = *(const LAS v2f*)&lds[L_GU + lane * 2], uu = *(const LAS v2f*)&lds[L_GU + 128 + lane * 2];
    const float h0 = rbf(silu_f(rbf(gg[0]))) * rbf(uu[0]), h1 = rbf(silu_f(rbf(gg[1]))) * rbf(uu[1]);
    lds[L_HB + lane] = cvtpk(h0, h1);
  }
  if (w == 3) {
    const auto rx = RSRC(xp, 2048);
    xres = __builtin_amdgcn_raw_buffer_load_b16(rx, ((h >> 4) * 512 + 64 * j + 4 * (h & 15) + (lane & 3)) * 2, 0, 0);
  }
  WGBAR();
  STAMP_AT(3);
  if (w < 2) {
    const auto ro = RSRC((const char*)wo + (size_t)h * 131072, 131072);
    v4u T[16];
    const int toff = (2 * r8) * 1024 + j * 128 + c8 * 16;
#pragma unroll
    for (int i = 0; i < 16; ++i) T[i] = LD16(ro, toff + ((i >> 1) * 16 + (i & 1)) * 1024, AUX_NT);
    WGBAR();
    STAMP_AT(4);
    const int goff = WS_X1 + (h * 8 + w * 4) * X1_STRIDE;
    u32 opair = 0;
    for (u32 spin = 0; spin < SPIN_CAP; ++spin) {
      v4u g[4], ml[4];
      asm volatile("" ::: "memory");
#pragma unroll
      for (int i = 0; i < 4; ++i) { g[i] = LD16(rws, goff + i * X1_STRIDE + lane * 16, AUX_SC1); ml[i] = LD16(rws, goff + i * X1_STRIDE + 1024, AUX_SC1); }
      u32 bad = 0;
#pragma unroll
      for (int i = 0; i < 4; ++i) bad |= (g[i][1] ^ tag) | (g[i][3] ^ tag) | (ml[i][1] ^ tag) | (ml[i][3] ^ tag);
      if (__all(bad == 0)) {
        const float M = fmaxf(fmaxf(bitsf(ml[0][0]), bitsf(ml[1][0])), fmaxf(bitsf(ml[2][0]), bitsf(ml[3][0])));
        float L = 0.f, o0 = 0.f, o1 = 0.f;
#pragma unroll
        for (int i = 0; i < 4; ++i) {
          const float wt = __builtin_amdgcn_exp2f((bitsf(ml[i][0]) - M) * 1.4426950408889634f);
          L = fmaf(wt, bitsf(ml[i][2]), L); o0 = fmaf(wt, bitsf(g[i][0]), o0); o1 = fmaf(wt, bitsf(g[i][2]), o1);
        }
        const float inv = __builtin_amdgcn_rcpf(L);
        opair = cvtpk(o0 * inv, o1 * inv);
        break;
      }
    }
    STAMP_AT(5);
    lds[L_OB + w * 64 + (lane & 7) * 8 + (lane >> 3)] = opair;
    asm volatile("s_waitcnt lgkmcnt(0)" ::: "memory");
    u32 cf[1][8];
    {
      const v4u a = *(const LAS v4u*)&lds[L_OB + w * 64 + r8 * 8], bq = *(const LAS v4u*)&lds[L_OB + w * 64 + r8 * 8 + 4];
#pragma unroll
      for (int e = 0; e < 4; ++e) { cf[0][e] = a[e]; cf[0][4 + e] = bq[e]; }
    }
    float acc[1][8];
#pragma unroll
    for (int e = 0; e < 8; ++e) acc[0][e] = 0.f;
    tile_gemv<8, 1>(T, cf, acc);
    float t[4];
#pragma unroll
    for (int e = 0; e < 4; ++e) t[e] = red32(acc[0][e], acc[0][e + 4]);
    const float u0 = red16(t[0], t[2]), u1 = red16(t[1], t[3]);
    const float a0 = u0 + dppf<XOR8>(u0), a1 = u1 + dppf<XOR8>(u1);
    const float y = (lane & 8) ? a1 : a0;
    const int n = c8 * 8 + (lane >> 5) * 4 + ((lane >> 4) & 1) * 2 + ((lane >> 3) & 1);
    ST8((v2u{fbits(y), tag}), rws, WS_X2 + id * 2048 + (w * 16 + (n >> 2)) * 64 + (n & 3) * 8);
    STAMP_AT(6);
    return;
  }
  if (w == 2) {
    const auto rd = RSRC(wd, 1376 * 1024);
    v4u T[6];
    const int row0 = 8 * ch0 + 2 * r8;
#pragma unroll
    for (int i = 0; i < 6; ++i) {
      const int rr = (i >> 1) * 16 + 2 * r8 + (i & 1);
      T[i] = LD16(rd, rr < nch * 8 ? (row0 + (i >> 1) * 16 + (i & 1)) * 1024 + j * 128 + c8 * 16 : 0x7ff00000, AUX_NT);
    }
    u32 cf[2][3];
#pragma unroll
    for (int bb = 0; bb < 2; ++bb)
#pragma unroll
      for (int k = 0; k < 3; ++k) cf[bb][k] = lds[L_HB + bb * 32 + 8 * k + r8];
    WGBAR();
    float acc[2][8];
#pragma unroll
    for (int bb = 0; bb < 2; ++bb)
#pragma unroll
      for (int e = 0; e < 8; ++e) acc[bb][e] = 0.f;
    tile_gemv<3, 2>(T, cf, acc);
    float t[8];
#pragma unroll
    for (int e = 0; e < 8; ++e) t[e] = red32(acc[0][e], acc[1][e]);
    float u[4];
#pragma unroll
    for (int e = 0; e < 4; ++e) u[e] = red16(t[e], t[e + 4]);
    const bool hi8 = (lane & 8) != 0;
    const float a0 = u[0] + dppf<XOR8>(u[0]), a1 = u[1] + dppf<XOR8>(u[1]);
    const float a2 = u[2] + dppf<XOR8>(u[2]), a3 = u[3] + dppf<XOR8>(u[3]);
    const float r0 = hi8 ? a2 : a0, r1 = hi8 ? a3 : a1;
    const int n = c8 * 8 + ((lane >> 4) & 1) * 4 + ((lane >> 3) & 1) * 2;
    ST16((v4u{fbits(r0), tag, fbits(r1), tag}), rws, WS_X2 + id * 2048 + ((lane >> 5) * 16 + (n >> 2)) * 64 + 32 + (n & 3) * 8);
    STAMP_AT(6);
    return;
  }
  WGBAR();
  STAMP_AT(4);
  {
    const v4f ml01 = *(const LAS v4f*)&lds[L_ML], ml23 = *(const LAS v4f*)&lds[L_ML + 4];
    v2f a[4];
#pragma unroll
    for (int i = 0; i < 4; ++i) a[i] = *(const LAS v2f*)&lds[L_RED + i * 128 + lane * 2];
    const float M = fmaxf(fmaxf(ml01[0], ml01[2]), fmaxf(ml23[0], ml23[2]));
    const float w0 = __builtin_amdgcn_exp2f((ml01[0] - M) * 1.4426950408889634f), w1 = __builtin_amdgcn_exp2f((ml01[2] - M) * 1.4426950408889634f);
    const float w2 = __builtin_amdgcn_exp2f((ml23[0] - M) * 1.4426950408889634f), w3 = __builtin_amdgcn_exp2f((ml23[2] - M) * 1.4426950408889634f);
    const float o0 = w0 * a[0][0] + w1 * a[1][0] + w2 * a[2][0] + w3 * a[3][0];
    const float o1 = w0 * a[0][1] + w1 * a[1][1] + w2 * a[2][1] + w3 * a[3][1];
    ST16((v4u{fbits(o0), tag, fbits(o1), tag}), rws, WS_X1 + id * X1_STRIDE + lane * 16);
    const float L = w0 * ml01[1] + w1 * ml01[3] + w2 * ml23[1] + w3 * ml23[3];
    if (lane == 0) ST16((v4u{fbits(M), tag, fbits(L), tag}), rws, WS_X1 + id * X1_STRIDE + 1024);
  }
  STAMP_AT(5);
  {
    const int goff = WS_X2 + ((lane & 31) * 8 + j) * 2048 + h * 64 + (lane >> 5) * 32;
    v4u g0, g1;
    for (u32 spin = 0; spin < SPIN_CAP; ++spin) {
      asm volatile("" ::: "memory");
      g0 = LD16(rws, goff, AUX_SC1); g1 = LD16(rws, goff + 16, AUX_SC1);
      const u32 bad = (g0[1] ^ tag) | (g0[3] ^ tag) | (g1[1] ^ tag) | (g1[3] ^ tag);
      if (__all(bad == 0)) break;
    }
    STAMP_AT(6);
    float v0 = bitsf(g0[0]), v1 = bitsf(g0[2]), v2 = bitsf(g1[0]), v3 = bitsf(g1[2]);
    const float e0 = v0 + dppf<XOR2>(v0), e1 = v1 + dppf<XOR2>(v1), e2 = v2 + dppf<XOR2>(v2), e3 = v3 + dppf<XOR2>(v3);
    const float f0 = (lane & 2) ? e2 : e0, f1 = (lane & 2) ? e3 : e1;
    const float k0 = f0 + dppf<XOR1>(f0), k1 = f1 + dppf<XOR1>(f1);
    float z = (lane & 1) ? k1 : k0;
    z += dppf<ROR4>(z); z += dppf<XOR8>(z);
    z = red16(z, z);
    auto sw2 = __builtin_amdgcn_permlane32_swap(fbits(z), fbits(z), false, false);
    const float ys = bitsf((u32)sw2[0]), ds = bitsf((u32)sw2[1]);
    const float gt = rbf(silu_f(rbf(ds)));
    const float res = bflo(xres) + rbf(rbf(ys) * gt);
    if (lane < 4) {
      const auto rout = RSRC(outp, 2048);
      __builtin_amdgcn_raw_buffer_store_b16((unsigned short)(cvtpk(res, res) & 0xffffu), rout, ((h >> 4) * 512 + 64 * j + 4 * (h & 15) + lane) * 2, 0, 0);
    }
    if (id == 0 && lane == 0) __builtin_amdgcn_raw_buffer_store_b32(tag, rws, WS_EPOCH, 0, AUX_SC1);
    STAMP_AT(7);
  }
}

extern "C" void kernel_launch(void* const* d_in, const int* in_sizes, int n_in, void* d_out, int out_size, void* d_ws,
                              size_t ws_size, hipStream_t stream) {
  fused_decode<<<256, 512, 0, stream>>>(d_in[0], d_in[1], d_in[2], d_in[3], d_in[4], d_in[6], d_in[7], d_in[8], d_out, d_ws);
}
```

```cpp
#include <hip/hip_runtime.h>
#include <stdint.h>

typedef unsigned u32;
typedef unsigned long long u64;
typedef u32 v4u __attribute__((ext_vector_type(4)));
typedef u32 v2u __attribute__((ext_vector_type(2)));
typedef float v2f __attribute__((ext_vector_type(2)));
typedef float v4f __attribute__((ext_vector_type(4)));
typedef short v4s __attribute__((ext_vector_type(4)));
typedef __bf16 bf2 __attribute__((ext_vector_type(2)));
typedef __bf16 bf8 __attribute__((ext_vector_type(8)));
#define LAS __attribute__((address_space(3)))

#define STAMP 0
#define NWAVES 8
#define WINL 10
#define PACE16 224
#define LEAD 7
#define SLEEP0 24
#define SLEEP1 40
#define SLEEP2 6
#define WPACE 300
#define HTIME 850

constexpr int SK = 8192, WIN = 2048;
constexpr int AUX_NT = 2, AUX_SC1 = 16;
constexpr float LOG2E = 1.4426950408889634f;
constexpr int WS_EPOCH = 0;
constexpr int WS_FLAG = 4096;
constexpr int WS_XH = 12288;
constexpr int WS_XQ = 24576;
constexpr int WS_XF = WS_XQ + 256 * 1024;
constexpr int WS_X1 = WS_XF + 344 * 512;
constexpr int WS_ML = WS_X1 + 256 * 512;
constexpr int WS_X2 = WS_ML + 256 * 128;
constexpr int WS_END = WS_X2 + 256 * 2048;
constexpr int WS_STAMP = 4 << 20;
constexpr u32 SPIN_CAP = 1u << 16;
constexpr int OOB = 0x7ff00000;

__device__ __forceinline__ float dot2(u32 a, u32 b, float c) {
  return __builtin_amdgcn_fdot2_f32_bf16(__builtin_bit_cast(bf2, a), __builtin_bit_cast(bf2, b), c, false);
}
__device__ __forceinline__ u32 cvtpk(float lo, float hi) {
  bf2 v = {(__bf16)lo, (__bf16)hi};
  return __builtin_bit_cast(u32, v);
}
__device__ __forceinline__ float rbf(float x) { return (float)(__bf16)x; }
__device__ __forceinline__ float bflo(u32 v) { return __builtin_bit_cast(float, v << 16); }
__device__ __forceinline__ float bfhi(u32 v) { return __builtin_bit_cast(float, v & 0xffff0000u); }
__device__ __forceinline__ u32 plo(u32 a, u32 b) { return __builtin_amdgcn_perm(b, a, 0x05040100u); }
__device__ __forceinline__ u32 phi(u32 a, u32 b) { return __builtin_amdgcn_perm(b, a, 0x07060302u); }
__device__ __forceinline__ u32 fbits(float f) { return __builtin_bit_cast(u32, f); }
__device__ __forceinline__ float bitsf(u32 u) { return __builtin_bit_cast(float, u); }
template <int CTRL> __device__ __forceinline__ float dppf(float x) {
  return __builtin_bit_cast(float, __builtin_amdgcn_mov_dpp(__builtin_bit_cast(int, x), CTRL, 0xf, 0xf, true));
}
template <int CTRL> __device__ __forceinline__ u32 dppu(u32 x) {
  return (u32)__builtin_amdgcn_mov_dpp((int)x, CTRL, 0xf, 0xf, true);
}
constexpr int XOR1 = 0xB1, XOR2 = 0x4E, XOR7 = 0x141, XOR8 = 0x128, ROR4 = 0x124;
__device__ __forceinline__ float red32(float a, float b) {
  auto s = __builtin_amdgcn_permlane32_swap(fbits(a), fbits(b), false, false);
  return bitsf((u32)s[0]) + bitsf((u32)s[1]);
}
__device__ __forceinline__ float red16(float a, float b) {
  auto s = __builtin_amdgcn_permlane16_swap(fbits(a), fbits(b), false, false);
  return bitsf((u32)s[0]) + bitsf((u32)s[1]);
}
__device__ __forceinline__ float wave_sum(float x) {
  x += dppf<XOR1>(x); x += dppf<XOR2>(x); x += dppf<XOR7>(x); x += dppf<XOR8>(x);
  x = red16(x, x); x = red32(x, x);
  return x;
}
__device__ __forceinline__ float wave_max(float x) {
  x = fmaxf(x, dppf<XOR1>(x)); x = fmaxf(x, dppf<XOR2>(x)); x = fmaxf(x, dppf<XOR7>(x)); x = fmaxf(x, dppf<XOR8>(x));
  auto s = __builtin_amdgcn_permlane16_swap(fbits(x), fbits(x), false, false);
  x = fmaxf(bitsf((u32)s[0]), bitsf((u32)s[1]));
  auto t = __builtin_amdgcn_permlane32_swap(fbits(x), fbits(x), false, false);
  return fmaxf(bitsf((u32)t[0]), bitsf((u32)t[1]));
}
__device__ __forceinline__ float silu_f(float x) { return x * __builtin_amdgcn_rcpf(1.0f + __builtin_amdgcn_exp2f(-LOG2E * x)); }
__device__ __forceinline__ v4f mfma16(v4u a, v4u b, v4f c) {
  return __builtin_amdgcn_mfma_f32_16x16x32_bf16(__builtin_bit_cast(bf8, a), __builtin_bit_cast(bf8, b), c, 0, 0, 0);
}
#define SB() __builtin_amdgcn_sched_barrier(0)
#define CFENCE() asm volatile("" ::: "memory")
#define RSRC(p, n) __builtin_amdgcn_make_buffer_rsrc((void*)(p), 0, (n), 0x00020000)
#define LD16(rs, voff, aux) __builtin_amdgcn_raw_buffer_load_b128((rs), (voff), 0, (aux))
#define LD16S(rs, voff, soff, aux) __builtin_amdgcn_raw_buffer_load_b128((rs), (voff), (soff), (aux))
#define ST16(v, rs, voff) __builtin_amdgcn_raw_buffer_store_b128((v), (rs), (voff), 0, AUX_SC1)
#define ST8(v, rs, voff) __builtin_amdgcn_raw_buffer_store_b64((v), (rs), (voff), 0, AUX_SC1)

#define STAMP_AT(k) do {} while (0)
#define PIN(x) do {} while (0)
#define STAMP_OUT() do {} while (0)

constexpr int C_PREP = 0, C_Q = 1, C_QP = 2, L_TAG = 8;
constexpr int L_Q = 16;
constexpr int L_QP = 80;
constexpr int L_XN = 208;
constexpr int L_ML = 720;
constexpr int L_RED = 736;
constexpr int L_HB = 1248;
constexpr int L_GM = 1312;
constexpr int L_A = 1328;
constexpr int L_W = 1872;
constexpr int L_END = L_W + 192 * 36;
#define WGBAR() do { SB(); asm volatile("s_waitcnt lgkmcnt(0)" ::: "memory"); __builtin_amdgcn_s_barrier(); CFENCE(); SB(); } while (0)

__device__ __forceinline__ void lds_post(LAS u32* c, int lane) {
  CFENCE();
  if (lane == 0) __hip_atomic_fetch_add(c, 1u, __ATOMIC_RELAXED, __HIP_MEMORY_SCOPE_WORKGROUP);
  CFENCE();
}
__device__ __forceinline__ void lds_wait(LAS u32* c, u32 want) {
#pragma nounroll
  for (u32 spin = 0; spin < (SPIN_CAP << 4); ++spin) {
    const u32 v = __builtin_amdgcn_readfirstlane(__hip_atomic_load(c, __ATOMIC_RELAXED, __HIP_MEMORY_SCOPE_WORKGROUP));
    if (v >= want) break;
    __builtin_amdgcn_s_sleep(1);
  }
  CFENCE();
}

__device__ __forceinline__ void pace(u32 t) {
  asm volatile("1:\n\ts_memrealtime vcc\n\ts_waitcnt lgkmcnt(0)\n\ts_sub_u32 vcc_lo, vcc_lo, %0\n\ts_cmp_lt_i32 vcc_lo, 0\n\ts_cbranch_scc1 1b"
               :: "s"(t) : "vcc", "scc");
}
__device__ __forceinline__ void pace_sleep(u32 t) {
  asm volatile("1:\n\ts_sleep 4\n\ts_memrealtime vcc\n\ts_waitcnt lgkmcnt(0)\n\ts_sub_u32 vcc_lo, vcc_lo, %0\n\ts_cmp_lt_i32 vcc_lo, 0\n\ts_cbranch_scc1 1b"
               :: "s"(t) : "vcc", "scc");
}

__global__ void __launch_bounds__(512, 2)
fused_decode(const void* __restrict__ xp, const void* __restrict__ kc, const void* __restrict__ vc,
             const void* __restrict__ wq, const void* __restrict__ wo, const void* __restrict__ wg,
             const void* __restrict__ wu, const void* __restrict__ wd, void* __restrict__ outp, void* __restrict__ ws) {
  __shared__ __attribute__((aligned(16))) u32 lds_[L_END];
  LAS u32* lds = (LAS u32*)lds_;
  const int c = blockIdx.x;
  const int h = (c & 7) * 4 + (c >> 6), j = (c >> 3) & 7, b = j >> 2, s = j & 3;
  const int id = h * 8 + j;
  const int lane = threadIdx.x & 63, wv = __builtin_amdgcn_readfirstlane(threadIdx.x >> 6);
  STAMP_AT(0);
  asm volatile("" ::"s"(xp), "s"(kc), "s"(vc), "s"(wq), "s"(wo), "s"(wg), "s"(wu), "s"(wd), "s"(outp), "s"(ws));
  const auto rws = RSRC(ws, WS_END);
  const u32 tb = (u32)__builtin_amdgcn_s_memrealtime();

  if (__builtin_expect(wv < 4, 1)) {
    const int sw = wv;
    const size_t kvoff = (((size_t)(b * 32 + h)) * SK + (SK - WIN) + s * 512) * 256;
    const auto rk = RSRC((const char*)kc + kvoff, 131072);
    const auto rv = RSRC((const char*)vc + kvoff, 131072);
    const int lin = sw * 4096 + lane * 16;
    v4u K[32], V[32];
#define THROTTLE(n) do { SB(); if (WINL) asm volatile("s_waitcnt vmcnt(%0)" :: "i"(WINL ? WINL - 1 : 0)); \
      if (PACE16) pace(tb + ((((n) + LEAD) * PACE16) >> 4)); SB(); } while (0)
#define K_ISSUE(i) do { if ((i) >= 6) THROTTLE(i); K[i] = LD16S(rk, lin + ((i) & 3) * 1024, ((i) >> 2) * 16384, AUX_NT); SB(); } while (0)
#define V_ISSUE(i) do { THROTTLE(32 + (i)); V[i] = LD16S(rv, lin + ((i) & 3) * 1024, ((i) >> 2) * 16384, AUX_NT); SB(); } while (0)
#pragma unroll
    for (int i = 0; i < 6; ++i) K_ISSUE(i);
    STAMP_AT(7);
    __builtin_amdgcn_s_barrier();
    CFENCE();
#pragma unroll
    for (int i = 6; i < 32; ++i) K_ISSUE(i);
#pragma unroll
    for (int i = 0; i < 8; ++i) V_ISSUE(i);
    STAMP_AT(1);
    lds_wait(lds + C_Q, 1);
    STAMP_AT(2);
    const int cc = lane & 15;
    const v4u q = *(const LAS v4u*)&lds[L_Q + cc * 4];
    const u32 tag = __builtin_amdgcn_readfirstlane(lds[L_TAG]);
    float x[2][16];
#pragma unroll
    for (int i = 0; i < 32; ++i) {
      float d = dot2(K[i][0], q[0], 0.f);
      d = dot2(K[i][1], q[1], d); d = dot2(K[i][2], q[2], d); d = dot2(K[i][3], q[3], d);
      x[i & 1][i >> 1] = d;
      if ((i & 7) == 7) V_ISSUE(8 + (i >> 3));
    }
    float sc[2];
#define TR_STEP(HALF, CTRL) _Pragma("unroll") for (int t = 0; t < (HALF); ++t) { \
      const float t1 = x[g][t] + dppf<CTRL>(x[g][t]), t2 = x[g][t + (HALF)] + dppf<CTRL>(x[g][t + (HALF)]); x[g][t] = (cc & (HALF)) ? t2 : t1; }
#pragma unroll
    for (int g = 0; g < 2; ++g) {
      TR_STEP(8, XOR8) TR_STEP(4, XOR7) TR_STEP(2, XOR2) TR_STEP(1, XOR1)
      sc[g] = rbf(rbf(x[g][0]) * 0.08838834764831845f);
      V_ISSUE(12 + g);
    }
#undef TR_STEP
    const float m = wave_max(fmaxf(sc[0], sc[1]));
    V_ISSUE(14);
    const float p0 = __builtin_amdgcn_exp2f((sc[0] - m) * LOG2E), p1 = __builtin_amdgcn_exp2f((sc[1] - m) * LOG2E);
    const float l = wave_sum(p0 + p1);
    const u32 pp = cvtpk(p0, p1);
    *(LAS v2f*)&lds[L_ML + sw * 2] = v2f{m, l};
    V_ISSUE(15);
    STAMP_AT(3);
    float acc[8];
#pragma unroll
    for (int e = 0; e < 8; ++e) acc[e] = 0.f;
#define PV_STEP(i) do { const u32 pb = dppu<0x150 + (i)>(pp); \
      _Pragma("unroll") for (int e = 0; e < 4; ++e) { \
        acc[2 * e] = dot2(plo(V[2 * (i)][e], V[2 * (i) + 1][e]), pb, acc[2 * e]); \
        acc[2 * e + 1] = dot2(phi(V[2 * (i)][e], V[2 * (i) + 1][e]), pb, acc[2 * e + 1]); } SB(); } while (0)
    PV_STEP(0); PV_STEP(1); V_ISSUE(16); PV_STEP(2); PV_STEP(3); V_ISSUE(17); PV_STEP(4); V_ISSUE(18); V_ISSUE(19);
    PV_STEP(5); V_ISSUE(20); V_ISSUE(21); PV_STEP(6); V_ISSUE(22); V_ISSUE(23); PV_STEP(7); V_ISSUE(24); V_ISSUE(25);
    PV_STEP(8); V_ISSUE(26); V_ISSUE(27); PV_STEP(9); V_ISSUE(28); V_ISSUE(29); PV_STEP(10); V_ISSUE(30); V_ISSUE(31);
    STAMP_AT(4);
    WGBAR();
    float f;
    {
      const v4f ml0 = *(const LAS v4f*)&lds[L_ML], ml1 = *(const LAS v4f*)&lds[L_ML + 4];
      const float M = fmaxf(fmaxf(ml0[0], ml0[2]), fmaxf(ml1[0], ml1[2]));
      f = __builtin_amdgcn_exp2f((m - M) * LOG2E);
      SB();
    }
    PV_STEP(11); PV_STEP(12); PV_STEP(13); PV_STEP(14); PV_STEP(15);
    PIN(acc[0]); PIN(acc[7]);
    STAMP_AT(5);
    float t[4];
#pragma unroll
    for (int e = 0; e < 4; ++e) t[e] = red32(acc[e], acc[e + 4]);
    const float u0 = red16(t[0], t[2]) * f, u1 = red16(t[1], t[3]) * f;
    const int R = lane >> 4;
    *(LAS v2f*)&lds[L_RED + sw * 128 + cc * 8 + (R >> 1) * 4 + (R & 1) * 2] = v2f{u0, u1};
    WGBAR();
    if (sw == 0) {
      const LAS v4f* rp = (const LAS v4f*)&lds[L_RED + (lane >> 5) * 256 + (lane & 31) * 4];
      const v4f a = rp[0] + rp[32];
      const float o0 = red32(a[0], a[0]), o1 = red32(a[1], a[1]), o2 = red32(a[2], a[2]), o3 = red32(a[3], a[3]);
      if (lane < 32) ST16((v4u{cvtpk(o0, o1), tag, cvtpk(o2, o3), tag}), rws, WS_X1 + id * 512 + lane * 16);
      STAMP_AT(6);
    }
    STAMP_OUT();
    return;
  }

  __builtin_amdgcn_s_setprio(3);
  const int w = wv - 4;
  const int r8 = lane >> 3, c8 = lane & 7;
  const int ch0 = (172 * h) >> 5, nch = ((172 * (h + 1)) >> 5) - ch0;
  const int ft = (w == 2) ? c : 256 + c;
  const bool has_tile = (w < 2) || (ft < 344);
  u32 tag;
  {
    const v4u xa = LD16(RSRC(w < 2 ? xp : ws, w < 2 ? 2048 : (w == 2 ? 16 : 0)), (w & 1) * 1024 + lane * 16, AUX_SC1);
    if (w == 3 && lane < 16) lds[lane] = 0u;
    const char* base = (const char*)(w < 2 ? wq : ((ft & 1) ? wu : wg));
    const int rstride = w < 2 ? 8192 : 5504;
    const int soff0 = w < 2 ? (128 * (j >> 1) + 64 * w) * 8192 + h * 256 + (j & 1) * 128 : ((ft >> 1) & 3) * 64 * 5504 + (ft >> 3) * 128;
    const auto rt = RSRC(base, has_tile ? (w < 2 ? 512 * 8192 : 256 * 5504) : 0);
    const int toff = 8 * r8 * rstride + c8 * 16;
    v4u T[8];
#pragma unroll
    for (int i = 0; i < 8; ++i) T[i] = LD16S(rt, toff, soff0 + i * rstride, AUX_NT);
    SB();
    STAMP_AT(8);
    asm volatile("s_waitcnt lgkmcnt(0)" ::: "memory");
    __builtin_amdgcn_s_barrier();
    CFENCE();
    if (w < 2) {
      float v[8];
#pragma unroll
      for (int e = 0; e < 4; ++e) { v[2 * e] = bflo(xa[e]); v[2 * e + 1] = bfhi(xa[e]); }
      const float sum = ((v[0] + v[1]) + (v[2] + v[3])) + ((v[4] + v[5]) + (v[6] + v[7]));
      const float mean = wave_sum(sum) * (1.0f / 512.0f);
      float sq = 0.f;
#pragma unroll
      for (int e = 0; e < 8; ++e) { const float d = v[e] - mean; sq = fmaf(d, d, sq); }
      const float var = wave_sum(sq) * (1.0f / 512.0f);
      const float mb = rbf(mean);
      const float den = rbf(__builtin_amdgcn_sqrtf(rbf(rbf(var) + 1e-5f)));
      const float inv = __builtin_amdgcn_rcpf(den);
      v4u xn;
#pragma unroll
      for (int e = 0; e < 4; ++e) {
        const u32 dd = cvtpk(v[2 * e] - mb, v[2 * e + 1] - mb);
        xn[e] = cvtpk(bflo(dd) * inv, bfhi(dd) * inv);
      }
      *(LAS v4u*)&lds[L_XN + w * 256 + lane * 4] = xn;
      lds_post(lds + C_PREP, lane);
    } else if (w == 2) {
      if (lane == 0) lds[L_TAG] = xa[0] + 1u;
      lds_post(lds + C_PREP, lane);
    }
    lds_wait(lds + C_PREP, 3);
    tag = __builtin_amdgcn_readfirstlane(lds[L_TAG]);
    u32 cf[2][4];
    if (w < 2) {
#pragma unroll
      for (int bb = 0; bb < 2; ++bb) {
        const v4u d = *(const LAS v4u*)&lds[L_XN + bb * 256 + (16 * (j >> 1) + 8 * w + r8) * 4];
#pragma unroll
        for (int k = 0; k < 4; ++k) cf[bb][k] = d[k];
      }
    } else {
      const u32 sel = ((ft >> 3) * 8 + c8 >= 172) ? 0x07060302u : 0x05040100u;
#pragma unroll
      for (int bb = 0; bb < 2; ++bb) {
        const LAS v4u* p = (const LAS v4u*)&lds[L_XN + bb * 256 + ((ft >> 1) & 3) * 64 + r8 * 8];
        const v4u d0 = p[0], d1 = p[1];
        cf[bb][0] = __builtin_amdgcn_perm(d0[1], d0[0], sel); cf[bb][1] = __builtin_amdgcn_perm(d0[3], d0[2], sel);
        cf[bb][2] = __builtin_amdgcn_perm(d1[1], d1[0], sel); cf[bb][3] = __builtin_amdgcn_perm(d1[3], d1[2], sel);
      }
    }
    STAMP_AT(9);
    float acc[2][8];
#pragma unroll
    for (int k = 0; k < 4; ++k)
#pragma unroll
      for (int e = 0; e < 4; ++e) {
        const u32 lo = plo(T[2 * k][e], T[2 * k + 1][e]), hi = phi(T[2 * k][e], T[2 * k + 1][e]);
#pragma unroll
        for (int bb = 0; bb < 2; ++bb) {
          acc[bb][2 * e] = dot2(lo, cf[bb][k], k ? acc[bb][2 * e] : 0.f);
          acc[bb][2 * e + 1] = dot2(hi, cf[bb][k], k ? acc[bb][2 * e + 1] : 0.f);
        }
      }
    float t[8];
#pragma unroll
    for (int e = 0; e < 8; ++e) t[e] = red32(acc[0][e], acc[1][e]);
    float u[4];
#pragma unroll
    for (int e = 0; e < 4; ++e) { u[e] = red16(t[e], t[e + 4]); u[e] += dppf<XOR8>(u[e]); }
    if (w < 2) {
      const bool hi8 = (lane & 8) != 0;
      float r0 = hi8 ? u[2] : u[0], r1 = hi8 ? u[3] : u[1];
      if (w == 1) {
        *(LAS v2f*)&lds[L_QP + lane * 2] = v2f{r0, r1};
        lds_post(lds + C_QP, lane);
      } else {
        lds_wait(lds + C_QP, 1);
        const v2f o = *(const LAS v2f*)&lds[L_QP + lane * 2];
        const int pos = (lane >> 5) * 64 + c8 * 8 + ((lane >> 4) & 1) * 4 + ((lane >> 3) & 1) * 2;
        ST16((v4u{fbits(r0 + o[0]), tag, fbits(r1 + o[1]), tag}), rws, WS_XQ + id * 1024 + pos * 8);
        const int goff = WS_XQ + (h * 8 + (lane >> 5)) * 1024 + b * 512 + (lane & 31) * 16;
        float s0 = 0.f, s1 = 0.f;
        __builtin_amdgcn_s_sleep(SLEEP0);
#pragma nounroll
        for (u32 spin = 0; spin < SPIN_CAP; ++spin) {
          CFENCE();
          v4u g[4];
#pragma unroll
          for (int i = 0; i < 4; ++i) g[i] = LD16(rws, goff + i * 2048, AUX_SC1);
          u32 bad = 0;
#pragma unroll
          for (int i = 0; i < 4; ++i) bad |= (g[i][1] ^ tag) | (g[i][3] ^ tag);
          s0 = (bitsf(g[0][0]) + bitsf(g[1][0])) + (bitsf(g[2][0]) + bitsf(g[3][0]));
          s1 = (bitsf(g[0][2]) + bitsf(g[1][2])) + (bitsf(g[2][2]) + bitsf(g[3][2]));
          if (__all(bad == 0)) break;
        }
        lds[L_Q + lane] = cvtpk(s0, s1);
        lds_post(lds + C_Q, lane);
      }
    } else {
      if (has_tile) {
        if (!(lane & 8)) ST16((v4u{fbits(u[0]), fbits(u[1]), fbits(u[2]), fbits(u[3])}), rws, WS_XF + ft * 512 + c8 * 64 + (lane >> 4) * 16);
        asm volatile("s_waitcnt vmcnt(0)" ::: "memory");
        if (lane == 0) __builtin_amdgcn_raw_buffer_store_b32(tag, rws, WS_FLAG + (ft >> 3) * 128 + (ft & 7) * 4, 0, AUX_SC1);
      }
      if (w == 3 && c >= 84) {
        const int uc = 255 - c, uu = uc + 172 * (lane >> 5), f = (uu >> 3) * 8 + ((lane >> 2) & 7);
        __builtin_amdgcn_s_sleep(SLEEP1);
#pragma nounroll
        for (u32 spin = 0; spin < SPIN_CAP; ++spin) {
          CFENCE();
          const u32 fl = __builtin_amdgcn_raw_buffer_load_b32(rws, WS_FLAG + (f >> 3) * 128 + (f & 7) * 4, 0, AUX_SC1);
          if (__all(fl == tag)) break;
          __builtin_amdgcn_s_sleep(8);
        }
        CFENCE();
        const v4u g = LD16(rws, WS_XF + f * 512 + (uu & 7) * 64 + (lane & 3) * 16, AUX_SC1);
        float hv[4];
#pragma unroll
        for (int e = 0; e < 4; ++e) {
          float z = bitsf(g[e]);
          z += dppf<XOR8>(z); z = red16(z, z); z = red32(z, z);
          hv[e] = rbf(silu_f(rbf(z))) * rbf(dppf<ROR4>(z));
        }
        if (lane < 4) ST16((v4u{cvtpk(hv[0], hv[1]), tag, cvtpk(hv[2], hv[3]), tag}), rws, WS_XH + uc * 64 + lane * 16);
      }
    }
  }
  STAMP_AT(2);
  __builtin_amdgcn_s_setprio(0);
  u32 xres = 0;
  if (w == 3) {
    xres = __builtin_amdgcn_raw_buffer_load_b16(RSRC(xp, 2048), ((h >> 4) * 512 + 64 * j + 4 * (h & 15) + (lane & 3)) * 2, 0, 0);
    asm volatile("" : "+v"(xres));
  }
  if (w == 2) {
    pace_sleep(tb + HTIME);
    const bool v = (lane >> 2) < nch;
    const int hoff = v ? WS_XH + ch0 * 64 + lane * 16 : OOB;
    const u32 etag = v ? tag : 0u;
    v4u hg;
#pragma nounroll
    for (u32 spin = 0; spin < SPIN_CAP; ++spin) {
      CFENCE();
      hg = LD16(rws, hoff, AUX_SC1);
      if (__all(((hg[1] ^ etag) | (hg[3] ^ etag)) == 0)) break;
    }
    if (lane < 32) *(LAS v2u*)&lds[L_HB + lane * 2] = v2u{hg[0], hg[2]};
  }
  STAMP_AT(1);
  WGBAR();
  STAMP_AT(3);
  const int g4 = lane >> 4;
  {
    const auto ro = RSRC((const char*)wo + (size_t)h * 131072, 131072);
    const auto rd = RSRC(wd, 1376 * 1024);
    v4u T[6];
#pragma unroll
    for (int i = 0; i < 4; ++i) {
      if (WPACE && i) pace(tb + (((63 + LEAD) * PACE16 + i * WPACE) >> 4));
      T[i] = LD16S(ro, (32 * w + r8) * 1024 + j * 128 + c8 * 16, i * 8192, AUX_NT);
      SB();
    }
#pragma unroll
    for (int i = 0; i < 2; ++i) T[4 + i] = LD16(rd, (2 * w + i < nch) ? (8 * (ch0 + 2 * w + i) + r8) * 1024 + j * 128 + c8 * 16 : OOB, AUX_NT);
    SB();
    if (w == 1) {
      const v4f ml0 = *(const LAS v4f*)&lds[L_ML], ml1 = *(const LAS v4f*)&lds[L_ML + 4];
      const float M = fmaxf(fmaxf(ml0[0], ml0[2]), fmaxf(ml1[0], ml1[2]));
      const float L = __builtin_amdgcn_exp2f((ml0[0] - M) * LOG2E) * ml0[1] + __builtin_amdgcn_exp2f((ml0[2] - M) * LOG2E) * ml0[3] +
                      __builtin_amdgcn_exp2f((ml1[0] - M) * LOG2E) * ml1[1] + __builtin_amdgcn_exp2f((ml1[2] - M) * LOG2E) * ml1[3];
      if (lane == 0) ST16((v4u{fbits(M), tag, fbits(L), tag}), rws, WS_ML + id * 128);
    }
    const v4u ha0 = *(const LAS v4u*)&lds[L_HB + g4 * 8 + ((lane >> 2) & 1) * 4], ha1 = *(const LAS v4u*)&lds[L_HB + 32 + g4 * 8 + ((lane >> 2) & 1) * 4];
    WGBAR();
    __builtin_amdgcn_s_sleep(SLEEP2);
    const int goff = WS_X1 + (h * 8 + 2 * w) * 512 + lane * 16;
    const int moff = WS_ML + (h * 8 + 2 * w + (lane >> 5)) * 128;
    v4u g = LD16(rws, goff, AUX_SC1), ml = LD16(rws, (lane & 31) == 0 ? moff : OOB, AUX_SC1);
    SB();
    STAMP_AT(7);
#pragma unroll
    for (int i = 0; i < 4; ++i) *(LAS v4u*)&lds[L_W + (32 * w + 8 * i + r8) * 36 + c8 * 4] = T[i];
#pragma unroll
    for (int i = 0; i < 2; ++i) *(LAS v4u*)&lds[L_W + (128 + 16 * w + 8 * i + r8) * 36 + c8 * 4] = T[4 + i];
    if (w != 3) STAMP_AT(8);
    WGBAR();
    v4u Bf[6];
    const int qq = (lane & 15) >> 2, pq = lane & 3;
    LAS u32* tp = lds + L_W + (8 * g4 + qq) * 36 + w * 8 + pq * 2;
#pragma unroll
    for (int t = 0; t < 6; ++t) {
      const v2u lo = __builtin_bit_cast(v2u, __builtin_amdgcn_ds_read_tr16_b64_v4i16((LAS v4s*)(tp + (32 * t) * 36)));
      const v2u hi = __builtin_bit_cast(v2u, __builtin_amdgcn_ds_read_tr16_b64_v4i16((LAS v4s*)(tp + (32 * t + 4) * 36)));
      Bf[t] = v4u{lo[0], lo[1], hi[0], hi[1]};
    }
    v4f dd = mfma16(ha0, Bf[4], v4f{0.f, 0.f, 0.f, 0.f});
    dd = mfma16(ha1, Bf[5], dd);
    float dsum = dd[0];
    asm volatile("" : "+v"(dsum), "+v"(Bf[0]), "+v"(Bf[1]), "+v"(Bf[2]), "+v"(Bf[3]));
    STAMP_AT(4);
    const u32 mtag = (lane & 31) == 0 ? tag : 0u;
#pragma nounroll
    for (u32 spin = 0; spin < SPIN_CAP; ++spin) {
      if (__all(((g[1] ^ tag) | (g[3] ^ tag) | (ml[1] ^ mtag) | (ml[3] ^ mtag)) == 0)) break;
      CFENCE();
      g = LD16(rws, goff, AUX_SC1); ml = LD16(rws, (lane & 31) == 0 ? moff : OOB, AUX_SC1);
    }
    *(LAS v2u*)&lds[L_A + (2 * w + (lane >> 5)) * 68 + (lane & 31) * 2] = v2u{g[0], g[2]};
    if ((lane & 31) == 0) *(LAS v2u*)&lds[L_GM + (2 * w + (lane >> 5)) * 2] = v2u{ml[0], ml[2]};
    STAMP_AT(5);
    WGBAR();
    const LAS v4u* ap = (const LAS v4u*)&lds[L_A + (lane & 7) * 68 + ((lane >> 3) & 1) * 32 + g4 * 4];
    const v4u ax = ap[0], ay = ap[4];
    const v4f s01 = *(const LAS v4f*)&lds[L_GM + (g4 & 1) * 8], s23 = *(const LAS v4f*)&lds[L_GM + (g4 & 1) * 8 + 4];
    v4f d1 = mfma16(ax, Bf[0], v4f{0.f, 0.f, 0.f, 0.f}), d2 = mfma16(ax, Bf[2], v4f{0.f, 0.f, 0.f, 0.f});
    d1 = mfma16(ay, Bf[1], d1); d2 = mfma16(ay, Bf[3], d2);
    const float M = fmaxf(fmaxf(s01[0], s01[2]), fmaxf(s23[0], s23[2]));
    const float w0 = __builtin_amdgcn_exp2f((s01[0] - M) * LOG2E), w1 = __builtin_amdgcn_exp2f((s01[2] - M) * LOG2E);
    const float w2 = __builtin_amdgcn_exp2f((s23[0] - M) * LOG2E), w3 = __builtin_amdgcn_exp2f((s23[2] - M) * LOG2E);
    const float inv = __builtin_amdgcn_rcpf((w0 * s01[1] + w1 * s01[3]) + (w2 * s23[1] + w3 * s23[3]));
    const float y1 = (w0 * d1[0] + w1 * d1[1]) + (w2 * d1[2] + w3 * d1[3]);
    const float y2 = (w0 * d2[0] + w1 * d2[1]) + (w2 * d2[2] + w3 * d2[3]);
    auto sw2 = __builtin_amdgcn_permlane32_swap(fbits(y2), fbits(y1), false, false);
    const float y = (y1 + bitsf((u32)sw2[1])) * inv;
    if (lane < 32) ST16((v4u{fbits(y), tag, fbits(dsum), tag}), rws, WS_X2 + id * 2048 + (g4 * 64 + w * 16 + (lane & 15)) * 16);
  }
  STAMP_AT(6);
  if (w == 3) {
    const int goff = WS_X2 + ((lane >> 2) * 8 + j) * 2048 + h * 64 + (lane & 3) * 16;
    v4u g0, g1;
#pragma nounroll
    for (u32 spin = 0; spin < SPIN_CAP; ++spin) {
      CFENCE();
      g0 = LD16(rws, goff, AUX_SC1); g1 = LD16S(rws, goff, 128 * 2048, AUX_SC1);
      const u32 bad = (g0[1] ^ tag) | (g0[3] ^ tag) | (g1[1] ^ tag) | (g1[3] ^ tag);
      if (__all(bad == 0)) break;
    }
    STAMP_AT(8);
    float ys = bitsf(g0[0]) + bitsf(g1[0]), ds = bitsf(g0[2]) + bitsf(g1[2]);
    ys += dppf<ROR4>(ys); ds += dppf<ROR4>(ds);
    ys += dppf<XOR8>(ys); ds += dppf<XOR8>(ds);
    float z = red16(ys, ds);
    z = red32(z, z);
    auto yd = __builtin_amdgcn_permlane16_swap(fbits(z), fbits(z), false, false);
    const float gt = rbf(silu_f(rbf(bitsf((u32)yd[1]))));
    const float res = bflo(xres) + rbf(rbf(bitsf((u32)yd[0])) * gt);
    if (lane < 4)
      __builtin_amdgcn_raw_buffer_store_b16((unsigned short)(cvtpk(res, res) & 0xffffu), RSRC(outp, 2048),
                                            ((h >> 4) * 512 + 64 * j + 4 * (h & 15) + lane) * 2, 0, 0);
    if (id == 0 && lane == 0) __builtin_amdgcn_raw_buffer_store_b32(tag, rws, WS_EPOCH, 0, AUX_SC1);
    STAMP_AT(9);
  }
  STAMP_OUT();
}

extern "C" void kernel_launch(void* const* d_in, const int* in_sizes, int n_in, void* d_out, int out_size, void* d_ws,
                              size_t ws_size, hipStream_t stream) {
  fused_decode<<<256, 512, 0, stream>>>(d_in[0], d_in[1], d_in[2], d_in[3], d_in[4], d_in[6], d_in[7], d_in[8], d_out, d_ws);
}
```

```cpp
#include <hip/hip_runtime.h>
#include <stdint.h>

typedef unsigned u32;
typedef unsigned long long u64;
typedef u32 v4u __attribute__((ext_vector_type(4)));
typedef u32 v2u __attribute__((ext_vector_type(2)));
typedef float v2f __attribute__((ext_vector_type(2)));
typedef float v4f __attribute__((ext_vector_type(4)));
typedef u32 v16u __attribute__((ext_vector_type(16)));
typedef short v4s __attribute__((ext_vector_type(4)));
typedef __bf16 bf2 __attribute__((ext_vector_type(2)));
typedef __bf16 bf8 __attribute__((ext_vector_type(8)));
#define LAS __attribute__((address_space(3)))

#define STAMP 0
#define NWAVES 8
#define WINL 7
#define PACE16 232
#define LEAD 8
#define BURST 6
#define SLEEP0 16
#define SLEEP1 40
#define SLEEP2 10
#define WPACE 300
#define PIPE2 1
#define SLEEP3 2
#define CODEPF 1
#define SLEEP4 4
#define QTIME 480
#define FTIME 600
#define MTIME 880
#define TPACE 188
#define TL0 110
#define HTIME 870

constexpr int SK = 8192, WIN = 2048;
constexpr int AUX_NT = 2, AUX_SC1 = 16;
constexpr float LOG2E = 1.4426950408889634f;
constexpr int WS_EPOCH = 0;
constexpr int WS_FLAG = 4096;
constexpr int WS_XH = 12288;
constexpr int WS_XQ = 24576;
constexpr int WS_XF = WS_XQ + 256 * 1024;
constexpr int WS_X1 = WS_XF + 344 * 512;
constexpr int WS_ML = WS_X1 + 256 * 512;
constexpr int WS_X2 = WS_ML + 256 * 128;
constexpr int WS_CLM = WS_X2 + 256 * 2048;
constexpr int WS_END = WS_CLM + 32 * 128;
constexpr int WS_STAMP = 4 << 20;
constexpr u32 SPIN_CAP = 1u << 16;
constexpr int OOB = 0x7ff00000;

__device__ __forceinline__ float dot2(u32 a, u32 b, float c) {
  return __builtin_amdgcn_fdot2_f32_bf16(__builtin_bit_cast(bf2, a), __builtin_bit_cast(bf2, b), c, false);
}
__device__ __forceinline__ u32 cvtpk(float lo, float hi) {
  bf2 v = {(__bf16)lo, (__bf16)hi};
  return __builtin_bit_cast(u32, v);
}
__device__ __forceinline__ float rbf(float x) { return (float)(__bf16)x; }
__device__ __forceinline__ float bflo(u32 v) { return __builtin_bit_cast(float, v << 16); }
__device__ __forceinline__ float bfhi(u32 v) { return __builtin_bit_cast(float, v & 0xffff0000u); }
__device__ __forceinline__ u32 plo(u32 a, u32 b) { return __builtin_amdgcn_perm(b, a, 0x05040100u); }
__device__ __forceinline__ u32 phi(u32 a, u32 b) { return __builtin_amdgcn_perm(b, a, 0x07060302u); }
__device__ __forceinline__ u32 fbits(float f) { return __builtin_bit_cast(u32, f); }
__device__ __forceinline__ float bitsf(u32 u) { return __builtin_bit_cast(float, u); }
template <int CTRL> __device__ __forceinline__ float dppf(float x) {
  return __builtin_bit_cast(float, __builtin_amdgcn_mov_dpp(__builtin_bit_cast(int, x), CTRL, 0xf, 0xf, true));
}
template <int CTRL> __device__ __forceinline__ u32 dppu(u32 x) {
  return (u32)__builtin_amdgcn_mov_dpp((int)x, CTRL, 0xf, 0xf, true);
}
constexpr int XOR1 = 0xB1, XOR2 = 0x4E, XOR7 = 0x141, XOR8 = 0x128, ROR4 = 0x124;
__device__ __forceinline__ float red32(float a, float b) {
  auto s = __builtin_amdgcn_permlane32_swap(fbits(a), fbits(b), false, false);
  return bitsf((u32)s[0]) + bitsf((u32)s[1]);
}
__device__ __forceinline__ float red16(float a, float b) {
  auto s = __builtin_amdgcn_permlane16_swap(fbits(a), fbits(b), false, false);
  return bitsf((u32)s[0]) + bitsf((u32)s[1]);
}
__device__ __forceinline__ float wave_sum(float x) {
  x += dppf<XOR1>(x); x += dppf<XOR2>(x); x += dppf<XOR7>(x); x += dppf<XOR8>(x);
  x = red16(x, x); x = red32(x, x);
  return x;
}
__device__ __forceinline__ float wave_max(float x) {
  x = fmaxf(x, dppf<XOR1>(x)); x = fmaxf(x, dppf<XOR2>(x)); x = fmaxf(x, dppf<XOR7>(x)); x = fmaxf(x, dppf<XOR8>(x));
  auto s = __builtin_amdgcn_permlane16_swap(fbits(x), fbits(x), false, false);
  x = fmaxf(bitsf((u32)s[0]), bitsf((u32)s[1]));
  auto t = __builtin_amdgcn_permlane32_swap(fbits(x), fbits(x), false, false);
  return fmaxf(bitsf((u32)t[0]), bitsf((u32)t[1]));
}
__device__ __forceinline__ float silu_f(float x) { return x * __builtin_amdgcn_rcpf(1.0f + __builtin_amdgcn_exp2f(-LOG2E * x)); }
__device__ __forceinline__ v4f mfma16(v4u a, v4u b, v4f c) {
  return __builtin_amdgcn_mfma_f32_16x16x32_bf16(__builtin_bit_cast(bf8, a), __builtin_bit_cast(bf8, b), c, 0, 0, 0);
}
#define SB() __builtin_amdgcn_sched_barrier(0)
#define CFENCE() asm volatile("" ::: "memory")
#define RSRC(p, n) __builtin_amdgcn_make_buffer_rsrc((void*)(p), 0, (n), 0x00020000)
#define LD16(rs, voff, aux) __builtin_amdgcn_raw_buffer_load_b128((rs), (voff), 0, (aux))
#define LD16S(rs, voff, soff, aux) __builtin_amdgcn_raw_buffer_load_b128((rs), (voff), (soff), (aux))
#define ST16(v, rs, voff) __builtin_amdgcn_raw_buffer_store_b128((v), (rs), (voff), 0, AUX_SC1)
#define ST8(v, rs, voff) __builtin_amdgcn_raw_buffer_store_b64((v), (rs), (voff), 0, AUX_SC1)

#define STAMP_AT(k) do {} while (0)
#define PIN(x) do {} while (0)
#define STAMP_OUT() do {} while (0)

constexpr int C_PREP = 0, C_Q = 1, C_QP = 2, C_ML = 3, L_TAG = 8, L_WIN = 9, L_F = 12;
constexpr int L_Q = 16;
constexpr int L_QP = 80;
constexpr int L_XN = 208;
constexpr int L_ML = 720;
constexpr int L_RED = 736;
constexpr int L_HB = 1248;
constexpr int L_GM = 1312;
constexpr int L_A = 1328;
constexpr int L_W = 1872;
constexpr int L_ST = L_W + 192 * 36;
constexpr int L_END = L_ST + 128;
#define WGBAR() do { SB(); asm volatile("s_waitcnt lgkmcnt(0)" ::: "memory"); __builtin_amdgcn_s_barrier(); CFENCE(); SB(); } while (0)

__device__ __forceinline__ void lds_post(LAS u32* c, int lane) {
  CFENCE();
  if (lane == 0) __hip_atomic_fetch_add(c, 1u, __ATOMIC_RELAXED, __HIP_MEMORY_SCOPE_WORKGROUP);
  CFENCE();
}
__device__ __forceinline__ void lds_wait(LAS u32* c, u32 want) {
#pragma nounroll
  for (u32 spin = 0; spin < (SPIN_CAP << 4); ++spin) {
    const u32 v = __builtin_amdgcn_readfirstlane(__hip_atomic_load(c, __ATOMIC_RELAXED, __HIP_MEMORY_SCOPE_WORKGROUP));
    if (v >= want) break;
    __builtin_amdgcn_s_sleep(1);
  }
  CFENCE();
}

__device__ __forceinline__ void pace(u32 t) {
  asm volatile("1:\n\ts_memrealtime vcc\n\ts_waitcnt lgkmcnt(0)\n\ts_sub_u32 vcc_lo, vcc_lo, %0\n\ts_cmp_lt_i32 vcc_lo, 0\n\ts_cbranch_scc1 1b"
               :: "s"(t) : "vcc", "scc");
}
__device__ __forceinline__ void pace_sleep(u32 t) {
  asm volatile("1:\n\ts_sleep 4\n\ts_memrealtime vcc\n\ts_waitcnt lgkmcnt(0)\n\ts_sub_u32 vcc_lo, vcc_lo, %0\n\ts_cmp_lt_i32 vcc_lo, 0\n\ts_cbranch_scc1 1b"
               :: "s"(t) : "vcc", "scc");
}

__global__ void __launch_bounds__(512, 2)
fused_decode(const void* a_x, const void* a_kc, const void* a_vc, const void* a_wq, const void* a_wo, const void* a_wg,
             const void* a_wu, const void* a_wd, void* a_out, void* a_ws) {
  v16u ka; v4u kb; u64 tb64;
  asm volatile("s_memrealtime %2\n\ts_load_dwordx16 %0, %3, 0x0\n\ts_load_dwordx4 %1, %3, 0x40"
               : "=&s"(ka), "=&s"(kb), "=&s"(tb64) : "s"(__builtin_amdgcn_kernarg_segment_ptr()));
  __shared__ __attribute__((aligned(16))) u32 lds_[L_END];
  LAS u32* lds = (LAS u32*)lds_;
  const int c = blockIdx.x;
  const int h = (c & 7) * 4 + (c >> 6), j = (c >> 3) & 7, b = j >> 2, s = j & 3;
  const int id = h * 8 + j;
  const int lane = threadIdx.x & 63, wv = __builtin_amdgcn_readfirstlane(threadIdx.x >> 6);
#define KARG(v, i) ((char*)(((u64)(v)[2 * (i) + 1] << 32) | (v)[2 * (i)]))
#define STAMP_INIT() do {} while (0)

  u32 cpf;
  {
    u64 pc; u32 len;
    asm volatile("s_getpc_b64 %0\nfd_code_here:\n\ts_mov_b32 %1, fd_code_end-fd_code_here" : "=s"(pc), "=s"(len));
    cpf = __builtin_amdgcn_raw_buffer_load_b32(RSRC((char*)pc, len), lane << 20, (c >> 3) * 512 + wv * 64 + 64, 0);
    SB();
  }
  const int kvoff = ((b * 32 + h) * SK + (SK - WIN) + s * 512) * 256 + wv * 4096;
  const int lin = lane * 16;
  lds[lane & 15] = 0u;
  SB();
  asm volatile("s_waitcnt lgkmcnt(0)" : "+s"(ka), "+s"(kb), "+s"(tb64) : "s"(kvoff), "v"(lin) : "memory");
  SB();
  const u32 tb = (u32)tb64;
  const auto rk = RSRC(KARG(ka, 1), 2 * 32 * SK * 256);
  v4u K[32];
  if (wv < 4) {
#pragma unroll
    for (int i = 0; i < BURST; ++i) { K[i] = LD16S(rk, lin + (i & 3) * 1024, kvoff + (i >> 2) * 16384, AUX_NT); SB(); }
  }
  STAMP_AT(8);
  CFENCE();
  __builtin_amdgcn_s_barrier();
  CFENCE();

  if (wv < 4) {
    const int sw = wv;
    const auto rv = RSRC(KARG(ka, 2), 2 * 32 * SK * 256);
    v4u V[32];
#define THROTTLE(n) do { SB(); if (WINL) asm volatile("s_waitcnt vmcnt(%0)" :: "i"(WINL ? WINL - 1 : 0)); \
      if (PACE16) pace(tb + ((((n) + LEAD) * PACE16) >> 4)); SB(); } while (0)
#define K_ISSUE(i) do { if ((i) >= BURST) THROTTLE(i); K[i] = LD16S(rk, lin + ((i) & 3) * 1024, kvoff + ((i) >> 2) * 16384, AUX_NT); SB(); } while (0)
#define V_ISSUE(i) do { THROTTLE(32 + (i)); V[i] = LD16S(rv, lin + ((i) & 3) * 1024, kvoff + ((i) >> 2) * 16384, AUX_NT); SB(); } while (0)
    STAMP_AT(6);
#pragma unroll
    for (int i = BURST; i < 32; ++i) K_ISSUE(i);
#pragma unroll
    for (int i = 0; i < 8; ++i) V_ISSUE(i);
    STAMP_AT(1);
    lds_wait(lds + C_Q, 1);
    asm volatile("" :: "v"(cpf));
    STAMP_AT(2);
    const int cc = lane & 15;
    const v4u q = *(const LAS v4u*)&lds[L_Q + cc * 4];
    float x[2][16];
#pragma unroll
    for (int i = 0; i < 32; ++i) {
      float d = dot2(K[i][0], q[0], 0.f);
      d = dot2(K[i][1], q[1], d); d = dot2(K[i][2], q[2], d); d = dot2(K[i][3], q[3], d);
      x[i & 1][i >> 1] = d;
      if ((i & 7) == 7) V_ISSUE(8 + (i >> 3));
    }
    float sc[2];
#define TR_STEP(HALF, CTRL) _Pragma("unroll") for (int t = 0; t < (HALF); ++t) { \
      const float t1 = x[g][t] + dppf<CTRL>(x[g][t]), t2 = x[g][t + (HALF)] + dppf<CTRL>(x[g][t + (HALF)]); x[g][t] = (cc & (HALF)) ? t2 : t1; }
#pragma unroll
    for (int g = 0; g < 2; ++g) {
      TR_STEP(8, XOR8) TR_STEP(4, XOR7) TR_STEP(2, XOR2) TR_STEP(1, XOR1)
      sc[g] = rbf(rbf(x[g][0]) * 0.08838834764831845f);
      V_ISSUE(12 + g);
    }
#undef TR_STEP
    const float m = wave_max(fmaxf(sc[0], sc[1]));
    V_ISSUE(14);
    const float p0 = __builtin_amdgcn_exp2f((sc[0] - m) * LOG2E), p1 = __builtin_amdgcn_exp2f((sc[1] - m) * LOG2E);
    const float l = wave_sum(p0 + p1);
    const u32 pp = cvtpk(p0, p1);
    *(LAS v2f*)&lds[L_ML + sw * 2] = v2f{m, l};
    asm volatile("s_mov_b64 exec, 1\n\tds_add_u32 %0, %1\n\ts_mov_b64 exec, -1" :: "v"((u32)(size_t)(lds + C_ML)), "v"(1u) : "memory");
    V_ISSUE(15);
    STAMP_AT(3);
    float acc[8];
#pragma unroll
    for (int e = 0; e < 8; ++e) acc[e] = 0.f;
#define PV_STEP(i) do { const u32 pb = dppu<0x150 + (i)>(pp); \
      _Pragma("unroll") for (int e = 0; e < 4; ++e) { \
        acc[2 * e] = dot2(plo(V[2 * (i)][e], V[2 * (i) + 1][e]), pb, acc[2 * e]); \
        acc[2 * e + 1] = dot2(phi(V[2 * (i)][e], V[2 * (i) + 1][e]), pb, acc[2 * e + 1]); } SB(); } while (0)
    PV_STEP(0); PV_STEP(1); V_ISSUE(16); PV_STEP(2); PV_STEP(3); V_ISSUE(17); PV_STEP(4); V_ISSUE(18); V_ISSUE(19);
    PV_STEP(5); V_ISSUE(20); V_ISSUE(21); PV_STEP(6); V_ISSUE(22); V_ISSUE(23); PV_STEP(7); V_ISSUE(24); V_ISSUE(25);
    PV_STEP(8); V_ISSUE(26); V_ISSUE(27); PV_STEP(9); V_ISSUE(28); V_ISSUE(29); PV_STEP(10); V_ISSUE(30); V_ISSUE(31);
    STAMP_AT(4);
    WGBAR();
    const float f = bitsf(lds[L_F + sw]);
    SB();
    PV_STEP(11); PV_STEP(12); PV_STEP(13); PV_STEP(14); PV_STEP(15);
    PIN(acc[0]); PIN(acc[7]);
    STAMP_AT(5);
    float t[4];
#pragma unroll
    for (int e = 0; e < 4; ++e) t[e] = red32(acc[e], acc[e + 4]);
    const float u0 = red16(t[0], t[2]) * f, u1 = red16(t[1], t[3]) * f;
    const int R = lane >> 4;
    *(LAS v2f*)&lds[L_RED + sw * 128 + cc * 8 + (R >> 1) * 4 + (R & 1) * 2] = v2f{u0, u1};
    WGBAR();
    asm volatile("fd_code_end:");
    return;
  }

  const int w = wv - 4;
  const int r8 = lane >> 3, c8 = lane & 7;
  const int ft = (w == 2) ? c : 256 + c;
  const bool has_tile = (w < 2) || (ft < 344);
  const int rstride = w < 2 ? 8192 : 5504;
  const int soff0 = w < 2 ? (128 * (j >> 1) + 64 * w) * 8192 + h * 256 + (j & 1) * 128 : ((ft >> 1) & 3) * 64 * 5504 + (ft >> 3) * 128;
  const int nrec = has_tile ? (w < 2 ? 512 * 8192 : 256 * 5504) : 0;
  const int toff = r8 * 8 * rstride + c8 * 16;
  const int xrec = w < 2 ? 2048 : (w == 2 ? 16 : 0), xoff = (w & 1) * 1024 + lane * 16;
  const char *xp = KARG(ka, 0), *wq = KARG(ka, 3), *wo = KARG(ka, 4), *wg = KARG(ka, 5), *wu = KARG(ka, 6), *wd = KARG(ka, 7);
  char *outp = KARG(kb, 0), *ws = KARG(kb, 1);
  STAMP_INIT();
  const auto rt = RSRC(w < 2 ? wq : ((ft & 1) ? wu : wg), nrec);
  const v4u xa = LD16(RSRC(w < 2 ? xp : ws, xrec), xoff, AUX_SC1);
  v4u T[8];
#pragma unroll
  for (int i = 0; i < 8; ++i) { if (TPACE) pace(tb + TL0 + ((i * TPACE) >> 4)); T[i] = LD16S(rt, toff, soff0 + i * rstride, AUX_NT); SB(); }
  const auto rws = RSRC(ws, WS_END);
  __builtin_amdgcn_s_setprio(3);
  const int ch0 = (172 * h) >> 5, nch = ((172 * (h + 1)) >> 5) - ch0;
  u32 tag;
  {
    if (w < 2) {
      float v[8];
#pragma unroll
      for (int e = 0; e < 4; ++e) { v[2 * e] = bflo(xa[e]); v[2 * e + 1] = bfhi(xa[e]); }
      const float sum = ((v[0] + v[1]) + (v[2] + v[3])) + ((v[4] + v[5]) + (v[6] + v[7]));
      const float mean = wave_sum(sum) * (1.0f / 512.0f);
      float sq = 0.f;
#pragma unroll
      for (int e = 0; e < 8; ++e) { const float d = v[e] - mean; sq = fmaf(d, d, sq); }
      const float var = wave_sum(sq) * (1.0f / 512.0f);
      const float mb = rbf(mean);
      const float den = rbf(__builtin_amdgcn_sqrtf(rbf(rbf(var) + 1e-5f)));
      const float inv = __builtin_amdgcn_rcpf(den);
      v4u xn;
#pragma unroll
      for (int e = 0; e < 4; ++e) {
        const u32 dd = cvtpk(v[2 * e] - mb, v[2 * e + 1] - mb);
        xn[e] = cvtpk(bflo(dd) * inv, bfhi(dd) * inv);
      }
      *(LAS v4u*)&lds[L_XN + w * 256 + lane * 4] = xn;
      lds_post(lds + C_PREP, lane);
    } else if (w == 2) {
      if (lane == 0) lds[L_TAG] = xa[0] == 0xffffffffu ? 1u : xa[0] + 1u;
      lds_post(lds + C_PREP, lane);
    }
    lds_wait(lds + C_PREP, 3);
    asm volatile("" :: "v"(cpf));
    tag = __builtin_amdgcn_readfirstlane(lds[L_TAG]);
    u32 cf[2][4];
    if (w < 2) {
#pragma unroll
      for (int bb = 0; bb < 2; ++bb) {
        const v4u d = *(const LAS v4u*)&lds[L_XN + bb * 256 + (16 * (j >> 1) + 8 * w + r8) * 4];
#pragma unroll
        for (int k = 0; k < 4; ++k) cf[bb][k] = d[k];
      }
    } else {
      const u32 sel = ((ft >> 3) * 8 + c8 >= 172) ? 0x07060302u : 0x05040100u;
#pragma unroll
      for (int bb = 0; bb < 2; ++bb) {
        const LAS v4u* p = (const LAS v4u*)&lds[L_XN + bb * 256 + ((ft >> 1) & 3) * 64 + r8 * 8];
        const v4u d0 = p[0], d1 = p[1];
        cf[bb][0] = __builtin_amdgcn_perm(d0[1], d0[0], sel); cf[bb][1] = __builtin_amdgcn_perm(d0[3], d0[2], sel);
        cf[bb][2] = __builtin_amdgcn_perm(d1[1], d1[0], sel); cf[bb][3] = __builtin_amdgcn_perm(d1[3], d1[2], sel);
      }
    }
    float acc[2][8];
#pragma unroll
    for (int k = 0; k < 4; ++k)
#pragma unroll
      for (int e = 0; e < 4; ++e) {
        const u32 lo = plo(T[2 * k][e], T[2 * k + 1][e]), hi = phi(T[2 * k][e], T[2 * k + 1][e]);
#pragma unroll
        for (int bb = 0; bb < 2; ++bb) {
          acc[bb][2 * e] = dot2(lo, cf[bb][k], k ? acc[bb][2 * e] : 0.f);
          acc[bb][2 * e + 1] = dot2(hi, cf[bb][k], k ? acc[bb][2 * e + 1] : 0.f);
        }
      }
    float t[8];
#pragma unroll
    for (int e = 0; e < 8; ++e) t[e] = red32(acc[0][e], acc[1][e]);
    float u[4];
#pragma unroll
    for (int e = 0; e < 4; ++e) { u[e] = red16(t[e], t[e + 4]); u[e] += dppf<XOR8>(u[e]); }
    if (w < 2) {
      const bool hi8 = (lane & 8) != 0;
      float r0 = hi8 ? u[2] : u[0], r1 = hi8 ? u[3] : u[1];
      if (w == 1) {
        *(LAS v2f*)&lds[L_QP + lane * 2] = v2f{r0, r1};
        lds_post(lds + C_QP, lane);
      } else {
        lds_wait(lds + C_QP, 1);
        const v2f o = *(const LAS v2f*)&lds[L_QP + lane * 2];
        const int pos = (lane >> 5) * 64 + c8 * 8 + ((lane >> 4) & 1) * 4 + ((lane >> 3) & 1) * 2;
        ST16((v4u{fbits(r0 + o[0]), tag, fbits(r1 + o[1]), tag}), rws, WS_XQ + id * 1024 + pos * 8);
        const int goff = WS_XQ + (h * 8 + (lane >> 5)) * 1024 + b * 512 + (lane & 31) * 16;
        float s0 = 0.f, s1 = 0.f;
        __builtin_amdgcn_s_sleep(SLEEP0);
        pace_sleep(tb + QTIME);
#pragma nounroll
        for (u32 spin = 0; spin < SPIN_CAP; ++spin) {
          CFENCE();
          v4u g[4];
#pragma unroll
          for (int i = 0; i < 4; ++i) g[i] = LD16(rws, goff + i * 2048, AUX_SC1);
          u32 bad = 0;
#pragma unroll
          for (int i = 0; i < 4; ++i) bad |= (g[i][1] ^ tag) | (g[i][3] ^ tag);
          s0 = (bitsf(g[0][0]) + bitsf(g[1][0])) + (bitsf(g[2][0]) + bitsf(g[3][0]));
          s1 = (bitsf(g[0][2]) + bitsf(g[1][2])) + (bitsf(g[2][2]) + bitsf(g[3][2]));
          if (__all(bad == 0)) break;
          __builtin_amdgcn_s_sleep(8);
        }
        lds[L_Q + lane] = cvtpk(s0, s1);
        lds_post(lds + C_Q, lane);
      }
    } else {
      if (has_tile) {
        if (!(lane & 8)) ST16((v4u{fbits(u[0]), fbits(u[1]), fbits(u[2]), fbits(u[3])}), rws, WS_XF + ft * 512 + c8 * 64 + (lane >> 4) * 16);
        asm volatile("s_waitcnt vmcnt(0)" ::: "memory");
        if (lane == 0) __builtin_amdgcn_raw_buffer_store_b32(tag, rws, WS_FLAG + (ft >> 3) * 128 + (ft & 7) * 4, 0, AUX_SC1);
      }
      if (w == 3 && c >= 84) {
        const int uc = 255 - c, uu = uc + 172 * (lane >> 5), f = (uu >> 3) * 8 + ((lane >> 2) & 7);
        pace_sleep(tb + FTIME);
#pragma nounroll
        for (u32 spin = 0; spin < SPIN_CAP; ++spin) {
          CFENCE();
          const u32 fl = __builtin_amdgcn_raw_buffer_load_b32(rws, WS_FLAG + (f >> 3) * 128 + (f & 7) * 4, 0, AUX_SC1);
          if (__all(fl == tag)) break;
          __builtin_amdgcn_s_sleep(8);
        }
        CFENCE();
        const v4u g = LD16(rws, WS_XF + f * 512 + (uu & 7) * 64 + (lane & 3) * 16, AUX_SC1);
        float hv[4];
#pragma unroll
        for (int e = 0; e < 4; ++e) {
          float z = bitsf(g[e]);
          z += dppf<XOR8>(z); z = red16(z, z); z = red32(z, z);
          hv[e] = rbf(silu_f(rbf(z))) * rbf(dppf<ROR4>(z));
        }
        if (lane < 4) ST16((v4u{cvtpk(hv[0], hv[1]), tag, cvtpk(hv[2], hv[3]), tag}), rws, WS_XH + uc * 64 + lane * 16);
      }
    }
  }
  STAMP_AT(2);
  __builtin_amdgcn_s_setprio(0);
  const int oidx = (c >> 7) * 512 + 64 * j + ((c >> 6) & 1) * 32 + w * 8 + c8;
  u32 xres = __builtin_amdgcn_raw_buffer_load_b16(RSRC(xp, 2048), oidx * 2, 0, 0);
  asm volatile("" : "+v"(xres));
  if (w == 2) {
    pace_sleep(tb + HTIME);
    const bool v = (lane >> 2) < nch;
    const int hoff = v ? WS_XH + ch0 * 64 + lane * 16 : OOB;
    const u32 etag = v ? tag : 0u;
    v4u hg;
#pragma nounroll
    for (u32 spin = 0; spin < SPIN_CAP; ++spin) {
      CFENCE();
      hg = LD16(rws, hoff, AUX_SC1);
      if (__all(((hg[1] ^ etag) | (hg[3] ^ etag)) == 0)) break;
    }
    if (lane < 32) *(LAS v2u*)&lds[L_HB + lane * 2] = v2u{hg[0], hg[2]};
  }
  if (w == 1) {
    pace_sleep(tb + 770);
    lds_wait(lds + C_ML, 4);
    const v2u me = *(const LAS v2u*)&lds[L_ML + (lane & 3) * 2];
    ST16((v4u{me[0], tag, me[1], tag}), rws, lane < 4 ? WS_ML + id * 128 + lane * 16 : OOB);
    __builtin_amdgcn_s_sleep(SLEEP0);
    pace_sleep(tb + MTIME);
    const int moff = WS_ML + (h * 8 + b * 4 + ((lane >> 2) & 3)) * 128 + (lane & 3) * 16;
    v4u g;
#pragma nounroll
    for (u32 spin = 0; spin < SPIN_CAP; ++spin) {
      CFENCE();
      g = LD16(rws, lane < 16 ? moff : OOB, AUX_SC1);
      if (__all(lane >= 16 || ((g[1] ^ tag) | (g[3] ^ tag)) == 0)) break;
      __builtin_amdgcn_s_sleep(4);
    }
    const float mi = bitsf(g[0]);
    float M = fmaxf(mi, dppf<XOR1>(mi)); M = fmaxf(M, dppf<XOR2>(M)); M = fmaxf(M, dppf<XOR7>(M)); M = fmaxf(M, dppf<XOR8>(M));
    const float e = __builtin_amdgcn_exp2f((mi - M) * LOG2E);
    float L = e * bitsf(g[2]);
    L += dppf<XOR1>(L); L += dppf<XOR2>(L); L += dppf<XOR7>(L); L += dppf<XOR8>(L);
    if ((lane >> 2) == s) lds[L_F + (lane & 3)] = fbits(e * __builtin_amdgcn_rcpf(L));
  }
  const int g4 = lane >> 4;
  int a_red = L_RED + g4 * 128 + w * 32 + c8 * 4;
  int a_x1st = lane < 8 ? WS_X1 + id * 512 + w * 128 + lane * 16 : OOB;
  int a_x1ld = WS_X1 + (h * 8 + 2 * w) * 512 + lane * 16;
  int a_aw = L_A + (2 * w + (lane >> 5)) * 68 + (lane & 31) * 2;
  int a_ar = L_A + (lane & 7) * 68 + ((lane >> 3) & 1) * 32 + g4 * 4;
  int a_y = lane < 32 ? WS_X2 + id * 2048 + (g4 * 64 + w * 16 + (lane & 15)) * 16 : OOB;
  int a_g = WS_X2 + (r8 * 8 + j) * 2048 + ((c >> 6) * 32 + w * 8 + c8) * 16;
  int a_o = lane < 8 ? oidx * 2 : OOB;
  asm volatile("" : "+v"(a_red), "+v"(a_x1st), "+v"(a_x1ld), "+v"(a_aw), "+v"(a_ar), "+v"(a_y), "+v"(a_g), "+v"(a_o));
  bool win;
  STAMP_AT(1);
  WGBAR();
  STAMP_AT(3);
  {
    const auto ro = RSRC((const char*)wo + (size_t)h * 131072, 131072);
    const auto rd = RSRC(wd, 1376 * 1024);
    u32 old = 0;
    if (w == 3 && lane == 0)
      old = __hip_atomic_exchange((__attribute__((address_space(1))) u32*)(ws + WS_CLM + (c >> 3) * 128), tag, __ATOMIC_RELAXED, __HIP_MEMORY_SCOPE_AGENT);
    v4u U[6];
#pragma unroll
    for (int i = 0; i < 4; ++i) {
      if (WPACE && i) pace(tb + (((63 + LEAD) * PACE16 + i * WPACE) >> 4));
      U[i] = LD16S(ro, (32 * w + r8) * 1024 + j * 128 + c8 * 16, i * 8192, AUX_NT);
      SB();
    }
#pragma unroll
    for (int i = 0; i < 2; ++i) U[4 + i] = LD16(rd, (2 * w + i < nch) ? (8 * (ch0 + 2 * w + i) + r8) * 1024 + j * 128 + c8 * 16 : OOB, AUX_NT);
    SB();
    const v4u ha0 = *(const LAS v4u*)&lds[L_HB + g4 * 8 + ((lane >> 2) & 1) * 4], ha1 = *(const LAS v4u*)&lds[L_HB + 32 + g4 * 8 + ((lane >> 2) & 1) * 4];
    WGBAR();
    {
      const v4f a = *(const LAS v4f*)&lds[a_red];
      const float e0 = red16(a[0], a[1]), e1 = red16(a[2], a[3]);
      const float f0 = red32(e0, e0), f1 = red32(e1, e1);
      auto p0 = __builtin_amdgcn_permlane16_swap(fbits(f0), fbits(f0), false, false);
      auto p1 = __builtin_amdgcn_permlane16_swap(fbits(f1), fbits(f1), false, false);
      ST16((v4u{cvtpk(bitsf((u32)p0[0]), bitsf((u32)p0[1])), tag, cvtpk(bitsf((u32)p1[0]), bitsf((u32)p1[1])), tag}), rws, a_x1st);
      SB();
      if (w == 0) STAMP_AT(9);
    }
    __builtin_amdgcn_s_sleep(SLEEP2);
    v4u g = LD16(rws, a_x1ld, AUX_SC1);
    SB();
    if (w != 0) STAMP_AT(9);
#pragma unroll
    for (int i = 0; i < 4; ++i) *(LAS v4u*)&lds[L_W + (32 * w + 8 * i + r8) * 36 + c8 * 4] = U[i];
#pragma unroll
    for (int i = 0; i < 2; ++i) *(LAS v4u*)&lds[L_W + (128 + 16 * w + 8 * i + r8) * 36 + c8 * 4] = U[4 + i];
    if (w == 3 && lane == 0) lds[L_WIN] = old;
    if (w != 3) STAMP_AT(8);
    WGBAR();
    win = __builtin_amdgcn_readfirstlane(lds[L_WIN]) != tag;
    v4u Bf[6];
    const int qq = (lane & 15) >> 2, pq = lane & 3;
    LAS u32* tp = lds + L_W + (8 * g4 + qq) * 36 + w * 8 + pq * 2;
#pragma unroll
    for (int t = 0; t < 6; ++t) {
      const v2u lo = __builtin_bit_cast(v2u, __builtin_amdgcn_ds_read_tr16_b64_v4i16((LAS v4s*)(tp + (32 * t) * 36)));
      const v2u hi = __builtin_bit_cast(v2u, __builtin_amdgcn_ds_read_tr16_b64_v4i16((LAS v4s*)(tp + (32 * t + 4) * 36)));
      Bf[t] = v4u{lo[0], lo[1], hi[0], hi[1]};
    }
    v4f dd = mfma16(ha0, Bf[4], v4f{0.f, 0.f, 0.f, 0.f});
    dd = mfma16(ha1, Bf[5], dd);
    float dsum = dd[0];
    asm volatile("" : "+v"(Bf[0]), "+v"(Bf[1]), "+v"(Bf[2]), "+v"(Bf[3]), "+v"(dsum));
    STAMP_AT(4);
#pragma nounroll
    for (u32 spin = 0; spin < SPIN_CAP; ++spin) {
      if (__all(((g[1] ^ tag) | (g[3] ^ tag)) == 0)) break;
      CFENCE();
      g = LD16(rws, a_x1ld, AUX_SC1);
    }
    *(LAS v2u*)&lds[a_aw] = v2u{g[0], g[2]};
    STAMP_AT(5);
    WGBAR();
    const LAS v4u* ap = (const LAS v4u*)&lds[a_ar];
    const v4u ax = ap[0], ay = ap[4];
    v4f d1 = mfma16(ax, Bf[0], v4f{0.f, 0.f, 0.f, 0.f}), d2 = mfma16(ax, Bf[2], v4f{0.f, 0.f, 0.f, 0.f});
    d1 = mfma16(ay, Bf[1], d1); d2 = mfma16(ay, Bf[3], d2);
    const float y1 = (d1[0] + d1[1]) + (d1[2] + d1[3]), y2 = (d2[0] + d2[1]) + (d2[2] + d2[3]);
    auto sw2 = __builtin_amdgcn_permlane32_swap(fbits(y2), fbits(y1), false, false);
    const float y = y1 + bitsf((u32)sw2[1]);
    ST16((v4u{fbits(y), tag, fbits(dsum), tag}), rws, a_y);
  }
  STAMP_AT(6);
  if (win) {
    __builtin_amdgcn_s_sleep(SLEEP4);
    v4u g[4];
#pragma nounroll
    for (u32 spin = 0; spin < SPIN_CAP; ++spin) {
      CFENCE();
#pragma unroll
      for (int i = 0; i < 4; ++i) g[i] = LD16S(rws, a_g, i * 64 * 2048, AUX_SC1);
      u32 bad = 0;
#pragma unroll
      for (int i = 0; i < 4; ++i) bad |= (g[i][1] ^ tag) | (g[i][3] ^ tag);
      if (__all(bad == 0)) break;
    }
    float ys = (bitsf(g[0][0]) + bitsf(g[1][0])) + (bitsf(g[2][0]) + bitsf(g[3][0]));
    float ds = (bitsf(g[0][2]) + bitsf(g[1][2])) + (bitsf(g[2][2]) + bitsf(g[3][2]));
    if (w == 3) STAMP_AT(8);
    ys += dppf<XOR8>(ys); ds += dppf<XOR8>(ds);
    float z = red16(ys, ds);
    z = red32(z, z);
    auto yd = __builtin_amdgcn_permlane16_swap(fbits(z), fbits(z), false, false);
    const float gt = rbf(silu_f(rbf(bitsf((u32)yd[1]))));
    const float res = bflo(xres) + rbf(rbf(bitsf((u32)yd[0])) * gt);
    __builtin_amdgcn_raw_buffer_store_b16((unsigned short)(cvtpk(res, res) & 0xffffu), RSRC(outp, 2048), a_o, 0, 0);
    if (c < 8 && w == 0 && lane == 0) __builtin_amdgcn_raw_buffer_store_b32(tag, rws, WS_EPOCH, 0, AUX_SC1);
    STAMP_AT(7);
  }
  STAMP_OUT();
}

extern "C" void kernel_launch(void* const* d_in, const int* in_sizes, int n_in, void* d_out, int out_size, void* d_ws,
                              size_t ws_size, hipStream_t stream) {
  fused_decode<<<256, 512, 0, stream>>>(d_in[0], d_in[1], d_in[2], d_in[3], d_in[4], d_in[6], d_in[7], d_in[8], d_out, d_ws);
}
```

```cpp
#define PROBE 1
#define PM 0
#include <hip/hip_runtime.h>
#include <stdint.h>

typedef unsigned u32;
typedef unsigned long long u64;
typedef u32 v4u __attribute__((ext_vector_type(4)));
typedef u32 v2u __attribute__((ext_vector_type(2)));
typedef float v2f __attribute__((ext_vector_type(2)));
typedef float v4f __attribute__((ext_vector_type(4)));
typedef u32 v16u __attribute__((ext_vector_type(16)));
typedef short v4s __attribute__((ext_vector_type(4)));
typedef __bf16 bf2 __attribute__((ext_vector_type(2)));
typedef __bf16 bf8 __attribute__((ext_vector_type(8)));
#define LAS __attribute__((address_space(3)))

#define STAMP 0
#define NWAVES 8
#define WINL 7
#define PACE16 232
#define LEAD 8
#define BURST 6
#define SLEEP0 16
#define SLEEP1 40
#define SLEEP2 10
#define WPACE 300
#define PIPE2 1
#define SLEEP3 2
#define CODEPF 1
#define SLEEP4 4
#define QTIME 480
#define FTIME 600
#define MTIME 880
#define TPACE 188
#define TL0 110
#define HTIME 870

constexpr int SK = 8192, WIN = 2048;
constexpr int AUX_NT = 2, AUX_SC1 = 16;
constexpr float LOG2E = 1.4426950408889634f;
constexpr int WS_EPOCH = 0;
constexpr int WS_FLAG = 4096;
constexpr int WS_XH = 12288;
constexpr int WS_XQ = 24576;
constexpr int WS_XF = WS_XQ + 256 * 1024;
constexpr int WS_X1 = WS_XF + 344 * 512;
constexpr int WS_ML = WS_X1 + 256 * 512;
constexpr int WS_X2 = WS_ML + 256 * 128;
constexpr int WS_CLM = WS_X2 + 256 * 2048;
constexpr int WS_END = WS_CLM + 32 * 128;
constexpr int WS_STAMP = 4 << 20;
constexpr u32 SPIN_CAP = 1u << 16;
constexpr int OOB = 0x7ff00000;

__device__ __forceinline__ float dot2(u32 a, u32 b, float c) {
  return __builtin_amdgcn_fdot2_f32_bf16(__builtin_bit_cast(bf2, a), __builtin_bit_cast(bf2, b), c, false);
}
__device__ __forceinline__ u32 cvtpk(float lo, float hi) {
  bf2 v = {(__bf16)lo, (__bf16)hi};
  return __builtin_bit_cast(u32, v);
}
__device__ __forceinline__ float rbf(float x) { return (float)(__bf16)x; }
__device__ __forceinline__ float bflo(u32 v) { return __builtin_bit_cast(float, v << 16); }
__device__ __forceinline__ float bfhi(u32 v) { return __builtin_bit_cast(float, v & 0xffff0000u); }
__device__ __forceinline__ u32 plo(u32 a, u32 b) { return __builtin_amdgcn_perm(b, a, 0x05040100u); }
__device__ __forceinline__ u32 phi(u32 a, u32 b) { return __builtin_amdgcn_perm(b, a, 0x07060302u); }
__device__ __forceinline__ u32 fbits(float f) { return __builtin_bit_cast(u32, f); }
__device__ __forceinline__ float bitsf(u32 u) { return __builtin_bit_cast(float, u); }
template <int CTRL> __device__ __forceinline__ float dppf(float x) {
  return __builtin_bit_cast(float, __builtin_amdgcn_mov_dpp(__builtin_bit_cast(int, x), CTRL, 0xf, 0xf, true));
}
template <int CTRL> __device__ __forceinline__ u32 dppu(u32 x) {
  return (u32)__builtin_amdgcn_mov_dpp((int)x, CTRL, 0xf, 0xf, true);
}
constexpr int XOR1 = 0xB1, XOR2 = 0x4E, XOR7 = 0x141, XOR8 = 0x128, ROR4 = 0x124;
__device__ __forceinline__ float red32(float a, float b) {
  auto s = __builtin_amdgcn_permlane32_swap(fbits(a), fbits(b), false, false);
  return bitsf((u32)s[0]) + bitsf((u32)s[1]);
}
__device__ __forceinline__ float red16(float a, float b) {
  auto s = __builtin_amdgcn_permlane16_swap(fbits(a), fbits(b), false, false);
  return bitsf((u32)s[0]) + bitsf((u32)s[1]);
}
__device__ __forceinline__ float wave_sum(float x) {
  x += dppf<XOR1>(x); x += dppf<XOR2>(x); x += dppf<XOR7>(x); x += dppf<XOR8>(x);
  x = red16(x, x); x = red32(x, x);
  return x;
}
__device__ __forceinline__ float wave_max(float x) {
  x = fmaxf(x, dppf<XOR1>(x)); x = fmaxf(x, dppf<XOR2>(x)); x = fmaxf(x, dppf<XOR7>(x)); x = fmaxf(x, dppf<XOR8>(x));
  auto s = __builtin_amdgcn_permlane16_swap(fbits(x), fbits(x), false, false);
  x = fmaxf(bitsf((u32)s[0]), bitsf((u32)s[1]));
  auto t = __builtin_amdgcn_permlane32_swap(fbits(x), fbits(x), false, false);
  return fmaxf(bitsf((u32)t[0]), bitsf((u32)t[1]));
}
__device__ __forceinline__ float silu_f(float x) { return x * __builtin_amdgcn_rcpf(1.0f + __builtin_amdgcn_exp2f(-LOG2E * x)); }
__device__ __forceinline__ v4f mfma16(v4u a, v4u b, v4f c) {
  return __builtin_amdgcn_mfma_f32_16x16x32_bf16(__builtin_bit_cast(bf8, a), __builtin_bit_cast(bf8, b), c, 0, 0, 0);
}
#define SB() __builtin_amdgcn_sched_barrier(0)
#define CFENCE() asm volatile("" ::: "memory")
#define RSRC(p, n) __builtin_amdgcn_make_buffer_rsrc((void*)(p), 0, (n), 0x00020000)
#define LD16(rs, voff, aux) __builtin_amdgcn_raw_buffer_load_b128((rs), (voff), 0, (aux))
#define LD16S(rs, voff, soff, aux) __builtin_amdgcn_raw_buffer_load_b128((rs), (voff), (soff), (aux))
#define ST16(v, rs, voff) __builtin_amdgcn_raw_buffer_store_b128((v), (rs), (voff), 0, AUX_SC1)
#define ST8(v, rs, voff) __builtin_amdgcn_raw_buffer_store_b64((v), (rs), (voff), 0, AUX_SC1)

#define STAMP_AT(k) do {} while (0)
#define PIN(x) do {} while (0)
#define STAMP_OUT() do {} while (0)

constexpr int C_PREP = 0, C_Q = 1, C_QP = 2, C_ML = 3, L_TAG = 8, L_WIN = 9, L_F = 12;
constexpr int L_Q = 16;
constexpr int L_QP = 80;
constexpr int L_XN = 208;
constexpr int L_ML = 720;
constexpr int L_RED = 736;
constexpr int L_HB = 1248;
constexpr int L_GM = 1312;
constexpr int L_A = 1328;
constexpr int L_W = 1872;
constexpr int L_ST = L_W + 192 * 36;
constexpr int L_END = L_ST + 128;
#define WGBAR() do { SB(); asm volatile("s_waitcnt lgkmcnt(0)" ::: "memory"); __builtin_amdgcn_s_barrier(); CFENCE(); SB(); } while (0)

__device__ __forceinline__ void lds_post(LAS u32* c, int lane) {
  CFENCE();
  if (lane == 0) __hip_atomic_fetch_add(c, 1u, __ATOMIC_RELAXED, __HIP_MEMORY_SCOPE_WORKGROUP);
  CFENCE();
}
__device__ __forceinline__ void lds_wait(LAS u32* c, u32 want) {
#pragma nounroll
  for (u32 spin = 0; spin < (SPIN_CAP << 4); ++spin) {
    const u32 v = __builtin_amdgcn_readfirstlane(__hip_atomic_load(c, __ATOMIC_RELAXED, __HIP_MEMORY_SCOPE_WORKGROUP));
    if (v >= want) break;
    __builtin_amdgcn_s_sleep(1);
  }
  CFENCE();
}

__device__ __forceinline__ void pace(u32 t) {
  asm volatile("1:\n\ts_memrealtime vcc\n\ts_waitcnt lgkmcnt(0)\n\ts_sub_u32 vcc_lo, vcc_lo, %0\n\ts_cmp_lt_i32 vcc_lo, 0\n\ts_cbranch_scc1 1b"
               :: "s"(t) : "vcc", "scc");
}
__device__ __forceinline__ void pace_sleep(u32 t) {
  asm volatile("1:\n\ts_sleep 4\n\ts_memrealtime vcc\n\ts_waitcnt lgkmcnt(0)\n\ts_sub_u32 vcc_lo, vcc_lo, %0\n\ts_cmp_lt_i32 vcc_lo, 0\n\ts_cbranch_scc1 1b"
               :: "s"(t) : "vcc", "scc");
}

__global__ void __launch_bounds__(512, 2)
fused_decode(const void* a_x, const void* a_kc, const void* a_vc, const void* a_wq, const void* a_wo, const void* a_wg,
             const void* a_wu, const void* a_wd, void* a_out, void* a_ws) {
  v16u ka; v4u kb; u64 tb64;
  asm volatile("s_memrealtime %2\n\ts_load_dwordx16 %0, %3, 0x0\n\ts_load_dwordx4 %1, %3, 0x40"
               : "=&s"(ka), "=&s"(kb), "=&s"(tb64) : "s"(__builtin_amdgcn_kernarg_segment_ptr()));
  __shared__ __attribute__((aligned(16))) u32 lds_[L_END];
  LAS u32* lds = (LAS u32*)lds_;
  const int c = blockIdx.x;
  const int h = (c & 7) * 4 + (c >> 6), j = (c >> 3) & 7, b = j >> 2, s = j & 3;
  const int id = h * 8 + j;
  const int lane = threadIdx.x & 63, wv = __builtin_amdgcn_readfirstlane(threadIdx.x >> 6);
#define KARG(v, i) ((char*)(((u64)(v)[2 * (i) + 1] << 32) | (v)[2 * (i)]))
#define STAMP_INIT() do {} while (0)

  u32 cpf;
  {
    u64 pc; u32 len;
    asm volatile("s_getpc_b64 %0\nfd_code_here:\n\ts_mov_b32 %1, fd_code_end-fd_code_here" : "=s"(pc), "=s"(len));
    cpf = __builtin_amdgcn_raw_buffer_load_b32(RSRC((char*)pc, len), lane << 20, (c >> 3) * 512 + wv * 64 + 64, 0);
    SB();
  }
  const int kvoff = ((b * 32 + h) * SK + (SK - WIN) + s * 512) * 256 + wv * 4096;
  const int lin = lane * 16;
  lds[lane & 15] = 0u;
  SB();
  asm volatile("s_waitcnt lgkmcnt(0)" : "+s"(ka), "+s"(kb), "+s"(tb64) : "s"(kvoff), "v"(lin) : "memory");
  SB();
  const u32 tb = (u32)tb64;
  const auto rk = RSRC(KARG(ka, 1), 2 * 32 * SK * 256);
  v4u K[32];
  if (wv < 4) {
#pragma unroll
    for (int i = 0; i < BURST; ++i) { K[i] = LD16S(rk, lin + (i & 3) * 1024, kvoff + (i >> 2) * 16384, AUX_NT); SB(); }
  }
  u32 px = 0;
  if (PROBE == 1) px = (u32)__builtin_amdgcn_s_memrealtime() - tb;
  STAMP_AT(8);
  CFENCE();
  __builtin_amdgcn_s_barrier();
  CFENCE();

  if (wv < 4) {
    const int sw = wv;
    const auto rv = RSRC(KARG(ka, 2), 2 * 32 * SK * 256);
    v4u V[32];
    if (PROBE == 2) { asm volatile("s_waitcnt vmcnt(%0)" :: "i"(BURST - 1) : "memory"); px = (u32)__builtin_amdgcn_s_memrealtime() - tb; }
#define THROTTLE(n) do { SB(); if (WINL) asm volatile("s_waitcnt vmcnt(%0)" :: "i"(WINL ? WINL - 1 : 0)); \
      if (PACE16) pace(tb + ((((n) + LEAD) * PACE16) >> 4)); SB(); } while (0)
#define K_ISSUE(i) do { if ((i) >= BURST) THROTTLE(i); K[i] = LD16S(rk, lin + ((i) & 3) * 1024, kvoff + ((i) >> 2) * 16384, AUX_NT); SB(); } while (0)
#define V_ISSUE(i) do { THROTTLE(32 + (i)); V[i] = LD16S(rv, lin + ((i) & 3) * 1024, kvoff + ((i) >> 2) * 16384, AUX_NT); SB(); } while (0)
    STAMP_AT(6);
#pragma unroll
    for (int i = BURST; i < 32; ++i) K_ISSUE(i);
#pragma unroll
    for (int i = 0; i < 8; ++i) V_ISSUE(i);
    STAMP_AT(1);
    lds_wait(lds + C_Q, 1);
    asm volatile("" :: "v"(cpf));
    STAMP_AT(2);
    const int cc = lane & 15;
    const v4u q = *(const LAS v4u*)&lds[L_Q + cc * 4];
    float x[2][16];
#pragma unroll
    for (int i = 0; i < 32; ++i) {
      float d = dot2(K[i][0], q[0], 0.f);
      d = dot2(K[i][1], q[1], d); d = dot2(K[i][2], q[2], d); d = dot2(K[i][3], q[3], d);
      x[i & 1][i >> 1] = d;
      if ((i & 7) == 7) V_ISSUE(8 + (i >> 3));
    }
    float sc[2];
#define TR_STEP(HALF, CTRL) _Pragma("unroll") for (int t = 0; t < (HALF); ++t) { \
      const float t1 = x[g][t] + dppf<CTRL>(x[g][t]), t2 = x[g][t + (HALF)] + dppf<CTRL>(x[g][t + (HALF)]); x[g][t] = (cc & (HALF)) ? t2 : t1; }
#pragma unroll
    for (int g = 0; g < 2; ++g) {
      TR_STEP(8, XOR8) TR_STEP(4, XOR7) TR_STEP(2, XOR2) TR_STEP(1, XOR1)
      sc[g] = rbf(rbf(x[g][0]) * 0.08838834764831845f);
      V_ISSUE(12 + g);
    }
#undef TR_STEP
    const float m = wave_max(fmaxf(sc[0], sc[1]));
    V_ISSUE(14);
    const float p0 = __builtin_amdgcn_exp2f((sc[0] - m) * LOG2E), p1 = __builtin_amdgcn_exp2f((sc[1] - m) * LOG2E);
    const float l = wave_sum(p0 + p1);
    const u32 pp = cvtpk(p0, p1);
    *(LAS v2f*)&lds[L_ML + sw * 2] = v2f{m, l};
    asm volatile("s_mov_b64 exec, 1\n\tds_add_u32 %0, %1\n\ts_mov_b64 exec, -1" :: "v"((u32)(size_t)(lds + C_ML)), "v"(1u) : "memory");
    V_ISSUE(15);
    STAMP_AT(3);
    float acc[8];
#pragma unroll
    for (int e = 0; e < 8; ++e) acc[e] = 0.f;
#define PV_STEP(i) do { const u32 pb = dppu<0x150 + (i)>(pp); \
      _Pragma("unroll") for (int e = 0; e < 4; ++e) { \
        acc[2 * e] = dot2(plo(V[2 * (i)][e], V[2 * (i) + 1][e]), pb, acc[2 * e]); \
        acc[2 * e + 1] = dot2(phi(V[2 * (i)][e], V[2 * (i) + 1][e]), pb, acc[2 * e + 1]); } SB(); } while (0)
    PV_STEP(0); PV_STEP(1); V_ISSUE(16); PV_STEP(2); PV_STEP(3); V_ISSUE(17); PV_STEP(4); V_ISSUE(18); V_ISSUE(19);
    PV_STEP(5); V_ISSUE(20); V_ISSUE(21); PV_STEP(6); V_ISSUE(22); V_ISSUE(23); PV_STEP(7); V_ISSUE(24); V_ISSUE(25);
    PV_STEP(8); V_ISSUE(26); V_ISSUE(27); PV_STEP(9); V_ISSUE(28); V_ISSUE(29); PV_STEP(10); V_ISSUE(30); V_ISSUE(31);
    if (PROBE == 5) px = (u32)__builtin_amdgcn_s_memrealtime() - tb;
    if (PROBE && sw == 0) lds[10] = px;
    STAMP_AT(4);
    WGBAR();
    const float f = bitsf(lds[L_F + sw]);
    SB();
    PV_STEP(11); PV_STEP(12); PV_STEP(13); PV_STEP(14); PV_STEP(15);
    PIN(acc[0]); PIN(acc[7]);
    STAMP_AT(5);
    float t[4];
#pragma unroll
    for (int e = 0; e < 4; ++e) t[e] = red32(acc[e], acc[e + 4]);
    const float u0 = red16(t[0], t[2]) * f, u1 = red16(t[1], t[3]) * f;
    const int R = lane >> 4;
    *(LAS v2f*)&lds[L_RED + sw * 128 + cc * 8 + (R >> 1) * 4 + (R & 1) * 2] = v2f{u0, u1};
    WGBAR();
    asm volatile("fd_code_end:");
    return;
  }

  const int w = wv - 4;
  const int r8 = lane >> 3, c8 = lane & 7;
  const int ft = (w == 2) ? c : 256 + c;
  const bool has_tile = (w < 2) || (ft < 344);
  const int rstride = w < 2 ? 8192 : 5504;
  const int soff0 = w < 2 ? (128 * (j >> 1) + 64 * w) * 8192 + h * 256 + (j & 1) * 128 : ((ft >> 1) & 3) * 64 * 5504 + (ft >> 3) * 128;
  const int nrec = has_tile ? (w < 2 ? 512 * 8192 : 256 * 5504) : 0;
  const int toff = r8 * 8 * rstride + c8 * 16;
  const int xrec = w < 2 ? 2048 : (w == 2 ? 16 : 0), xoff = (w & 1) * 1024 + lane * 16;
  const char *xp = KARG(ka, 0), *wq = KARG(ka, 3), *wo = KARG(ka, 4), *wg = KARG(ka, 5), *wu = KARG(ka, 6), *wd = KARG(ka, 7);
  char *outp = KARG(kb, 0), *ws = KARG(kb, 1);
  STAMP_INIT();
  const auto rt = RSRC(w < 2 ? wq : ((ft & 1) ? wu : wg), nrec);
  const v4u xa = LD16(RSRC(w < 2 ? xp : ws, xrec), xoff, AUX_SC1);
  v4u T[8];
#pragma unroll
  for (int i = 0; i < 8; ++i) { if (TPACE) pace(tb + TL0 + ((i * TPACE) >> 4)); T[i] = LD16S(rt, toff, soff0 + i * rstride, AUX_NT); SB(); }
  const auto rws = RSRC(ws, WS_END);
  __builtin_amdgcn_s_setprio(3);
  const int ch0 = (172 * h) >> 5, nch = ((172 * (h + 1)) >> 5) - ch0;
  u32 tag;
  {
    if (w < 2) {
      float v[8];
#pragma unroll
      for (int e = 0; e < 4; ++e) { v[2 * e] = bflo(xa[e]); v[2 * e + 1] = bfhi(xa[e]); }
      const float sum = ((v[0] + v[1]) + (v[2] + v[3])) + ((v[4] + v[5]) + (v[6] + v[7]));
      const float mean = wave_sum(sum) * (1.0f / 512.0f);
      float sq = 0.f;
#pragma unroll
      for (int e = 0; e < 8; ++e) { const float d = v[e] - mean; sq = fmaf(d, d, sq); }
      const float var = wave_sum(sq) * (1.0f / 512.0f);
      const float mb = rbf(mean);
      const float den = rbf(__builtin_amdgcn_sqrtf(rbf(rbf(var) + 1e-5f)));
      const float inv = __builtin_amdgcn_rcpf(den);
      v4u xn;
#pragma unroll
      for (int e = 0; e < 4; ++e) {
        const u32 dd = cvtpk(v[2 * e] - mb, v[2 * e + 1] - mb);
        xn[e] = cvtpk(bflo(dd) * inv, bfhi(dd) * inv);
      }
      *(LAS v4u*)&lds[L_XN + w * 256 + lane * 4] = xn;
      lds_post(lds + C_PREP, lane);
    } else if (w == 2) {
      if (lane == 0) lds[L_TAG] = xa[0] == 0xffffffffu ? 1u : xa[0] + 1u;
      lds_post(lds + C_PREP, lane);
    }
    lds_wait(lds + C_PREP, 3);
    asm volatile("" :: "v"(cpf));
    tag = __builtin_amdgcn_readfirstlane(lds[L_TAG]);
    u32 cf[2][4];
    if (w < 2) {
#pragma unroll
      for (int bb = 0; bb < 2; ++bb) {
        const v4u d = *(const LAS v4u*)&lds[L_XN + bb * 256 + (16 * (j >> 1) + 8 * w + r8) * 4];
#pragma unroll
        for (int k = 0; k < 4; ++k) cf[bb][k] = d[k];
      }
    } else {
      const u32 sel = ((ft >> 3) * 8 + c8 >= 172) ? 0x07060302u : 0x05040100u;
#pragma unroll
      for (int bb = 0; bb < 2; ++bb) {
        const LAS v4u* p = (const LAS v4u*)&lds[L_XN + bb * 256 + ((ft >> 1) & 3) * 64 + r8 * 8];
        const v4u d0 = p[0], d1 = p[1];
        cf[bb][0] = __builtin_amdgcn_perm(d0[1], d0[0], sel); cf[bb][1] = __builtin_amdgcn_perm(d0[3], d0[2], sel);
        cf[bb][2] = __builtin_amdgcn_perm(d1[1], d1[0], sel); cf[bb][3] = __builtin_amdgcn_perm(d1[3], d1[2], sel);
      }
    }
    float acc[2][8];
#pragma unroll
    for (int k = 0; k < 4; ++k)
#pragma unroll
      for (int e = 0; e < 4; ++e) {
        const u32 lo = plo(T[2 * k][e], T[2 * k + 1][e]), hi = phi(T[2 * k][e], T[2 * k + 1][e]);
#pragma unroll
        for (int bb = 0; bb < 2; ++bb) {
          acc[bb][2 * e] = dot2(lo, cf[bb][k], k ? acc[bb][2 * e] : 0.f);
          acc[bb][2 * e + 1] = dot2(hi, cf[bb][k], k ? acc[bb][2 * e + 1] : 0.f);
        }
      }
    float t[8];
#pragma unroll
    for (int e = 0; e < 8; ++e) t[e] = red32(acc[0][e], acc[1][e]);
    float u[4];
#pragma unroll
    for (int e = 0; e < 4; ++e) { u[e] = red16(t[e], t[e + 4]); u[e] += dppf<XOR8>(u[e]); }
    if (w < 2) {
      const bool hi8 = (lane & 8) != 0;
      float r0 = hi8 ? u[2] : u[0], r1 = hi8 ? u[3] : u[1];
      if (w == 1) {
        *(LAS v2f*)&lds[L_QP + lane * 2] = v2f{r0, r1};
        lds_post(lds + C_QP, lane);
      } else {
        lds_wait(lds + C_QP, 1);
        const v2f o = *(const LAS v2f*)&lds[L_QP + lane * 2];
        const int pos = (lane >> 5) * 64 + c8 * 8 + ((lane >> 4) & 1) * 4 + ((lane >> 3) & 1) * 2;
        ST16((v4u{fbits(r0 + o[0]), tag, fbits(r1 + o[1]), tag}), rws, WS_XQ + id * 1024 + pos * 8);
        const int goff = WS_XQ + (h * 8 + (lane >> 5)) * 1024 + b * 512 + (lane & 31) * 16;
        float s0 = 0.f, s1 = 0.f;
        __builtin_amdgcn_s_sleep(SLEEP0);
        pace_sleep(tb + QTIME);
#pragma nounroll
        for (u32 spin = 0; spin < SPIN_CAP; ++spin) {
          CFENCE();
          v4u g[4];
#pragma unroll
          for (int i = 0; i < 4; ++i) g[i] = LD16(rws, goff + i * 2048, AUX_SC1);
          u32 bad = 0;
#pragma unroll
          for (int i = 0; i < 4; ++i) bad |= (g[i][1] ^ tag) | (g[i][3] ^ tag);
          s0 = (bitsf(g[0][0]) + bitsf(g[1][0])) + (bitsf(g[2][0]) + bitsf(g[3][0]));
          s1 = (bitsf(g[0][2]) + bitsf(g[1][2])) + (bitsf(g[2][2]) + bitsf(g[3][2]));
          if (__all(bad == 0)) break;
          __builtin_amdgcn_s_sleep(8);
        }
        lds[L_Q + lane] = cvtpk(s0, s1);
        lds_post(lds + C_Q, lane);
      }
    } else {
      if (has_tile) {
        if (!(lane & 8)) ST16((v4u{fbits(u[0]), fbits(u[1]), fbits(u[2]), fbits(u[3])}), rws, WS_XF + ft * 512 + c8 * 64 + (lane >> 4) * 16);
        asm volatile("s_waitcnt vmcnt(0)" ::: "memory");
        if (lane == 0) __builtin_amdgcn_raw_buffer_store_b32(tag, rws, WS_FLAG + (ft >> 3) * 128 + (ft & 7) * 4, 0, AUX_SC1);
      }
      if (w == 3 && c >= 84) {
        const int uc = 255 - c, uu = uc + 172 * (lane >> 5), f = (uu >> 3) * 8 + ((lane >> 2) & 7);
        pace_sleep(tb + FTIME);
#pragma nounroll
        for (u32 spin = 0; spin < SPIN_CAP; ++spin) {
          CFENCE();
          const u32 fl = __builtin_amdgcn_raw_buffer_load_b32(rws, WS_FLAG + (f >> 3) * 128 + (f & 7) * 4, 0, AUX_SC1);
          if (__all(fl == tag)) break;
          __builtin_amdgcn_s_sleep(8);
        }
        CFENCE();
        const v4u g = LD16(rws, WS_XF + f * 512 + (uu & 7) * 64 + (lane & 3) * 16, AUX_SC1);
        float hv[4];
#pragma unroll
        for (int e = 0; e < 4; ++e) {
          float z = bitsf(g[e]);
          z += dppf<XOR8>(z); z = red16(z, z); z = red32(z, z);
          hv[e] = rbf(silu_f(rbf(z))) * rbf(dppf<ROR4>(z));
        }
        if (lane < 4) ST16((v4u{cvtpk(hv[0], hv[1]), tag, cvtpk(hv[2], hv[3]), tag}), rws, WS_XH + uc * 64 + lane * 16);
      }
    }
  }
  STAMP_AT(2);
  __builtin_amdgcn_s_setprio(0);
  const int oidx = (c >> 7) * 512 + 64 * j + ((c >> 6) & 1) * 32 + w * 8 + c8;
  u32 xres = __builtin_amdgcn_raw_buffer_load_b16(RSRC(xp, 2048), oidx * 2, 0, 0);
  asm volatile("" : "+v"(xres));
  if (w == 2) {
    pace_sleep(tb + HTIME);
    const bool v = (lane >> 2) < nch;
    const int hoff = v ? WS_XH + ch0 * 64 + lane * 16 : OOB;
    const u32 etag = v ? tag : 0u;
    v4u hg;
#pragma nounroll
    for (u32 spin = 0; spin < SPIN_CAP; ++spin) {
      CFENCE();
      hg = LD16(rws, hoff, AUX_SC1);
      if (__all(((hg[1] ^ etag) | (hg[3] ^ etag)) == 0)) break;
    }
    if (lane < 32) *(LAS v2u*)&lds[L_HB + lane * 2] = v2u{hg[0], hg[2]};
  }
  if (w == 1) {
    pace_sleep(tb + 770);
    lds_wait(lds + C_ML, 4);
    const v2u me = *(const LAS v2u*)&lds[L_ML + (lane & 3) * 2];
    ST16((v4u{me[0], tag, me[1], tag}), rws, lane < 4 ? WS_ML + id * 128 + lane * 16 : OOB);
    __builtin_amdgcn_s_sleep(SLEEP0);
    pace_sleep(tb + MTIME);
    const int moff = WS_ML + (h * 8 + b * 4 + ((lane >> 2) & 3)) * 128 + (lane & 3) * 16;
    v4u g;
#pragma nounroll
    for (u32 spin = 0; spin < SPIN_CAP; ++spin) {
      CFENCE();
      g = LD16(rws, lane < 16 ? moff : OOB, AUX_SC1);
      if (__all(lane >= 16 || ((g[1] ^ tag) | (g[3] ^ tag)) == 0)) break;
      __builtin_amdgcn_s_sleep(4);
    }
    const float mi = bitsf(g[0]);
    float M = fmaxf(mi, dppf<XOR1>(mi)); M = fmaxf(M, dppf<XOR2>(M)); M = fmaxf(M, dppf<XOR7>(M)); M = fmaxf(M, dppf<XOR8>(M));
    const float e = __builtin_amdgcn_exp2f((mi - M) * LOG2E);
    float L = e * bitsf(g[2]);
    L += dppf<XOR1>(L); L += dppf<XOR2>(L); L += dppf<XOR7>(L); L += dppf<XOR8>(L);
    if ((lane >> 2) == s) lds[L_F + (lane & 3)] = fbits(e * __builtin_amdgcn_rcpf(L));
  }
  const int g4 = lane >> 4;
  int a_red = L_RED + g4 * 128 + w * 32 + c8 * 4;
  int a_x1st = lane < 8 ? WS_X1 + id * 512 + w * 128 + lane * 16 : OOB;
  int a_x1ld = WS_X1 + (h * 8 + 2 * w) * 512 + lane * 16;
  int a_aw = L_A + (2 * w + (lane >> 5)) * 68 + (lane & 31) * 2;
  int a_ar = L_A + (lane & 7) * 68 + ((lane >> 3) & 1) * 32 + g4 * 4;
  int a_y = lane < 32 ? WS_X2 + id * 2048 + (g4 * 64 + w * 16 + (lane & 15)) * 16 : OOB;
  int a_g = WS_X2 + (r8 * 8 + j) * 2048 + ((c >> 6) * 32 + w * 8 + c8) * 16;
  int a_o = lane < 8 ? oidx * 2 : OOB;
  asm volatile("" : "+v"(a_red), "+v"(a_x1st), "+v"(a_x1ld), "+v"(a_aw), "+v"(a_ar), "+v"(a_y), "+v"(a_g), "+v"(a_o));
  bool win;
  STAMP_AT(1);
  WGBAR();
  STAMP_AT(3);
  {
    const auto ro = RSRC((const char*)wo + (size_t)h * 131072, 131072);
    const auto rd = RSRC(wd, 1376 * 1024);
    u32 old = 0;
    if (w == 3 && lane == 0)
      old = __hip_atomic_exchange((__attribute__((address_space(1))) u32*)(ws + WS_CLM + (c >> 3) * 128), tag, __ATOMIC_RELAXED, __HIP_MEMORY_SCOPE_AGENT);
    v4u U[6];
#pragma unroll
    for (int i = 0; i < 4; ++i) {
      if (WPACE && i) pace(tb + (((63 + LEAD) * PACE16 + i * WPACE) >> 4));
      U[i] = LD16S(ro, (32 * w + r8) * 1024 + j * 128 + c8 * 16, i * 8192, AUX_NT);
      SB();
    }
#pragma unroll
    for (int i = 0; i < 2; ++i) U[4 + i] = LD16(rd, (2 * w + i < nch) ? (8 * (ch0 + 2 * w + i) + r8) * 1024 + j * 128 + c8 * 16 : OOB, AUX_NT);
    SB();
    const v4u ha0 = *(const LAS v4u*)&lds[L_HB + g4 * 8 + ((lane >> 2) & 1) * 4], ha1 = *(const LAS v4u*)&lds[L_HB + 32 + g4 * 8 + ((lane >> 2) & 1) * 4];
    WGBAR();
    if (PROBE == 3) px = (u32)__builtin_amdgcn_s_memrealtime() - tb; else px = __builtin_amdgcn_readfirstlane(lds[10]);
    {
      const v4f a = *(const LAS v4f*)&lds[a_red];
      const float e0 = red16(a[0], a[1]), e1 = red16(a[2], a[3]);
      const float f0 = red32(e0, e0), f1 = red32(e1, e1);
      auto p0 = __builtin_amdgcn_permlane16_swap(fbits(f0), fbits(f0), false, false);
      auto p1 = __builtin_amdgcn_permlane16_swap(fbits(f1), fbits(f1), false, false);
      ST16((v4u{cvtpk(bitsf((u32)p0[0]), bitsf((u32)p0[1])), tag, cvtpk(bitsf((u32)p1[0]), bitsf((u32)p1[1])), tag}), rws, a_x1st);
      SB();
      if (w == 0) STAMP_AT(9);
    }
    __builtin_amdgcn_s_sleep(SLEEP2);
    v4u g = LD16(rws, a_x1ld, AUX_SC1);
    SB();
    if (w != 0) STAMP_AT(9);
#pragma unroll
    for (int i = 0; i < 4; ++i) *(LAS v4u*)&lds[L_W + (32 * w + 8 * i + r8) * 36 + c8 * 4] = U[i];
#pragma unroll
    for (int i = 0; i < 2; ++i) *(LAS v4u*)&lds[L_W + (128 + 16 * w + 8 * i + r8) * 36 + c8 * 4] = U[4 + i];
    if (w == 3 && lane == 0) lds[L_WIN] = old;
    if (w != 3) STAMP_AT(8);
    WGBAR();
    win = __builtin_amdgcn_readfirstlane(lds[L_WIN]) != tag;
    v4u Bf[6];
    const int qq = (lane & 15) >> 2, pq = lane & 3;
    LAS u32* tp = lds + L_W + (8 * g4 + qq) * 36 + w * 8 + pq * 2;
#pragma unroll
    for (int t = 0; t < 6; ++t) {
      const v2u lo = __builtin_bit_cast(v2u, __builtin_amdgcn_ds_read_tr16_b64_v4i16((LAS v4s*)(tp + (32 * t) * 36)));
      const v2u hi = __builtin_bit_cast(v2u, __builtin_amdgcn_ds_read_tr16_b64_v4i16((LAS v4s*)(tp + (32 * t + 4) * 36)));
      Bf[t] = v4u{lo[0], lo[1], hi[0], hi[1]};
    }
    v4f dd = mfma16(ha0, Bf[4], v4f{0.f, 0.f, 0.f, 0.f});
    dd = mfma16(ha1, Bf[5], dd);
    float dsum = dd[0];
    asm volatile("" : "+v"(Bf[0]), "+v"(Bf[1]), "+v"(Bf[2]), "+v"(Bf[3]), "+v"(dsum));
    STAMP_AT(4);
#pragma nounroll
    for (u32 spin = 0; spin < SPIN_CAP; ++spin) {
      if (__all(((g[1] ^ tag) | (g[3] ^ tag)) == 0)) break;
      CFENCE();
      g = LD16(rws, a_x1ld, AUX_SC1);
    }
    *(LAS v2u*)&lds[a_aw] = v2u{g[0], g[2]};
    STAMP_AT(5);
    WGBAR();
    const LAS v4u* ap = (const LAS v4u*)&lds[a_ar];
    const v4u ax = ap[0], ay = ap[4];
    v4f d1 = mfma16(ax, Bf[0], v4f{0.f, 0.f, 0.f, 0.f}), d2 = mfma16(ax, Bf[2], v4f{0.f, 0.f, 0.f, 0.f});
    d1 = mfma16(ay, Bf[1], d1); d2 = mfma16(ay, Bf[3], d2);
    const float y1 = (d1[0] + d1[1]) + (d1[2] + d1[3]), y2 = (d2[0] + d2[1]) + (d2[2] + d2[3]);
    auto sw2 = __builtin_amdgcn_permlane32_swap(fbits(y2), fbits(y1), false, false);
    const float y = y1 + bitsf((u32)sw2[1]);
    ST16((v4u{fbits(y), tag, fbits(dsum), tag}), rws, a_y);
  }
  STAMP_AT(6);
  if (win) {
    __builtin_amdgcn_s_sleep(SLEEP4);
    v4u g[4];
#pragma nounroll
    for (u32 spin = 0; spin < SPIN_CAP; ++spin) {
      CFENCE();
#pragma unroll
      for (int i = 0; i < 4; ++i) g[i] = LD16S(rws, a_g, i * 64 * 2048, AUX_SC1);
      u32 bad = 0;
#pragma unroll
      for (int i = 0; i < 4; ++i) bad |= (g[i][1] ^ tag) | (g[i][3] ^ tag);
      if (__all(bad == 0)) break;
    }
    float ys = (bitsf(g[0][0]) + bitsf(g[1][0])) + (bitsf(g[2][0]) + bitsf(g[3][0]));
    float ds = (bitsf(g[0][2]) + bitsf(g[1][2])) + (bitsf(g[2][2]) + bitsf(g[3][2]));
    if (w == 3) STAMP_AT(8);
    ys += dppf<XOR8>(ys); ds += dppf<XOR8>(ds);
    float z = red16(ys, ds);
    z = red32(z, z);
    auto yd = __builtin_amdgcn_permlane16_swap(fbits(z), fbits(z), false, false);
    const float gt = rbf(silu_f(rbf(bitsf((u32)yd[1]))));
    const float res = bflo(xres) + rbf(rbf(bitsf((u32)yd[0])) * gt);
    __builtin_amdgcn_raw_buffer_store_b16((unsigned short)(cvtpk(res, res) & 0xffffu), RSRC(outp, 2048), a_o, 0, 0);
    if (c < 8 && w == 0 && lane == 0) __builtin_amdgcn_raw_buffer_store_b32(tag, rws, WS_EPOCH, 0, AUX_SC1);
    STAMP_AT(7);
  }
  if (PROBE) {
    const u32 te = (u32)__builtin_amdgcn_s_memrealtime();
    if (PROBE == 4) px = te - tb;
    pace_sleep(tb + 2000 + PM * px);
  }
  STAMP_OUT();
}

extern "C" void kernel_launch(void* const* d_in, const int* in_sizes, int n_in, void* d_out, int out_size, void* d_ws,
                              size_t ws_size, hipStream_t stream) {
  fused_decode<<<256, 512, 0, stream>>>(d_in[0], d_in[1], d_in[2], d_in[3], d_in[4], d_in[6], d_in[7], d_in[8], d_out, d_ws);
}
```

```cpp
#include <hip/hip_runtime.h>
#include <stdint.h>

typedef unsigned u32;
typedef unsigned long long u64;
typedef u32 v4u __attribute__((ext_vector_type(4)));
typedef u32 v2u __attribute__((ext_vector_type(2)));
typedef float v2f __attribute__((ext_vector_type(2)));
typedef float v4f __attribute__((ext_vector_type(4)));
typedef u32 v16u __attribute__((ext_vector_type(16)));
typedef short v4s __attribute__((ext_vector_type(4)));
typedef __bf16 bf2 __attribute__((ext_vector_type(2)));
typedef __bf16 bf8 __attribute__((ext_vector_type(8)));
#define LAS __attribute__((address_space(3)))

#define STAMP 0
#define NWAVES 8
#define WINL 7
#define PACE16 232
#define LEAD 8
#define BURST 6
#define SLEEP0 16
#define SLEEP1 40
#define SLEEP2 10
#define WPACE 300
#define PIPE2 1
#define SLEEP3 2
#define CODEPF 1
#define SLEEP4 4
#define QTIME 480
#define FTIME 600
#define MTIME 880
#define TPACE 188
#define TL0 110
#define KA 30
#define BUCKET 0
#define WREL 0
#define TOUCH 0
#define T0NOW 1
#define ETOUCH 2
#define HTIME 870

constexpr int SK = 8192, WIN = 2048;
constexpr int AUX_NT = 2, AUX_SC1 = 16;
constexpr float LOG2E = 1.4426950408889634f;
constexpr int WS_EPOCH = 0;
constexpr int WS_FLAG = 4096;
constexpr int WS_XH = 12288;
constexpr int WS_XQ = 24576;
constexpr int WS_XF = WS_XQ + 256 * 1024;
constexpr int WS_X1 = WS_XF + 344 * 512;
constexpr int WS_ML = WS_X1 + 256 * 512;
constexpr int WS_X2 = WS_ML + 256 * 128;
constexpr int WS_CLM = WS_X2 + 256 * 2048;
constexpr int WS_END = WS_CLM + 32 * 128;
constexpr int WS_STAMP = 4 << 20;
constexpr u32 SPIN_CAP = 1u << 16;
constexpr int OOB = 0x7ff00000;

__device__ __forceinline__ float dot2(u32 a, u32 b, float c) {
  return __builtin_amdgcn_fdot2_f32_bf16(__builtin_bit_cast(bf2, a), __builtin_bit_cast(bf2, b), c, false);
}
__device__ __forceinline__ u32 cvtpk(float lo, float hi) {
  bf2 v = {(__bf16)lo, (__bf16)hi};
  return __builtin_bit_cast(u32, v);
}
__device__ __forceinline__ float rbf(float x) { return (float)(__bf16)x; }
__device__ __forceinline__ float bflo(u32 v) { return __builtin_bit_cast(float, v << 16); }
__device__ __forceinline__ float bfhi(u32 v) { return __builtin_bit_cast(float, v & 0xffff0000u); }
__device__ __forceinline__ u32 plo(u32 a, u32 b) { return __builtin_amdgcn_perm(b, a, 0x05040100u); }
__device__ __forceinline__ u32 phi(u32 a, u32 b) { return __builtin_amdgcn_perm(b, a, 0x07060302u); }
__device__ __forceinline__ u32 fbits(float f) { return __builtin_bit_cast(u32, f); }
__device__ __forceinline__ float bitsf(u32 u) { return __builtin_bit_cast(float, u); }
template <int CTRL> __device__ __forceinline__ float dppf(float x) {
  return __builtin_bit_cast(float, __builtin_amdgcn_mov_dpp(__builtin_bit_cast(int, x), CTRL, 0xf, 0xf, true));
}
template <int CTRL> __device__ __forceinline__ u32 dppu(u32 x) {
  return (u32)__builtin_amdgcn_mov_dpp((int)x, CTRL, 0xf, 0xf, true);
}
constexpr int XOR1 = 0xB1, XOR2 = 0x4E, XOR7 = 0x141, XOR8 = 0x128, ROR4 = 0x124;
__device__ __forceinline__ float red32(float a, float b) {
  auto s = __builtin_amdgcn_permlane32_swap(fbits(a), fbits(b), false, false);
  return bitsf((u32)s[0]) + bitsf((u32)s[1]);
}
__device__ __forceinline__ float red16(float a, float b) {
  auto s = __builtin_amdgcn_permlane16_swap(fbits(a), fbits(b), false, false);
  return bitsf((u32)s[0]) + bitsf((u32)s[1]);
}
__device__ __forceinline__ float wave_sum(float x) {
  x += dppf<XOR1>(x); x += dppf<XOR2>(x); x += dppf<XOR7>(x); x += dppf<XOR8>(x);
  x = red16(x, x); x = red32(x, x);
  return x;
}
__device__ __forceinline__ float wave_max(float x) {
  x = fmaxf(x, dppf<XOR1>(x)); x = fmaxf(x, dppf<XOR2>(x)); x = fmaxf(x, dppf<XOR7>(x)); x = fmaxf(x, dppf<XOR8>(x));
  auto s = __builtin_amdgcn_permlane16_swap(fbits(x), fbits(x), false, false);
  x = fmaxf(bitsf((u32)s[0]), bitsf((u32)s[1]));
  auto t = __builtin_amdgcn_permlane32_swap(fbits(x), fbits(x), false, false);
  return fmaxf(bitsf((u32)t[0]), bitsf((u32)t[1]));
}
__device__ __forceinline__ float silu_f(float x) { return x * __builtin_amdgcn_rcpf(1.0f + __builtin_amdgcn_exp2f(-LOG2E * x)); }
__device__ __forceinline__ v4f mfma16(v4u a, v4u b, v4f c) {
  return __builtin_amdgcn_mfma_f32_16x16x32_bf16(__builtin_bit_cast(bf8, a), __builtin_bit_cast(bf8, b), c, 0, 0, 0);
}
#define SB() __builtin_amdgcn_sched_barrier(0)
#define CFENCE() asm volatile("" ::: "memory")
#define RSRC(p, n) __builtin_amdgcn_make_buffer_rsrc((void*)(p), 0, (n), 0x00020000)
#define LD16(rs, voff, aux) __builtin_amdgcn_raw_buffer_load_b128((rs), (voff), 0, (aux))
#define LD16S(rs, voff, soff, aux) __builtin_amdgcn_raw_buffer_load_b128((rs), (voff), (soff), (aux))
#define ST16(v, rs, voff) __builtin_amdgcn_raw_buffer_store_b128((v), (rs), (voff), 0, AUX_SC1)
#define ST8(v, rs, voff) __builtin_amdgcn_raw_buffer_store_b64((v), (rs), (voff), 0, AUX_SC1)

#define STAMP_AT(k) do {} while (0)
#define PIN(x) do {} while (0)
#define STAMP_OUT() do {} while (0)

constexpr int C_PREP = 0, C_Q = 1, C_QP = 2, C_ML = 3, L_TAG = 8, L_WIN = 9, L_F = 12;
constexpr int L_Q = 16;
constexpr int L_QP = 80;
constexpr int L_XN = 208;
constexpr int L_ML = 720;
constexpr int L_RED = 736;
constexpr int L_HB = 1248;
constexpr int L_GM = 1312;
constexpr int L_A = 1328;
constexpr int L_W = 1872;
constexpr int L_ST = L_W + 192 * 36;
constexpr int L_END = L_ST + 128;
#define WGBAR() do { SB(); asm volatile("s_waitcnt lgkmcnt(0)" ::: "memory"); __builtin_amdgcn_s_barrier(); CFENCE(); SB(); } while (0)

__device__ __forceinline__ void lds_post(LAS u32* c, int lane) {
  CFENCE();
  if (lane == 0) __hip_atomic_fetch_add(c, 1u, __ATOMIC_RELAXED, __HIP_MEMORY_SCOPE_WORKGROUP);
  CFENCE();
}
__device__ __forceinline__ void lds_wait(LAS u32* c, u32 want) {
#pragma nounroll
  for (u32 spin = 0; spin < (SPIN_CAP << 4); ++spin) {
    const u32 v = __builtin_amdgcn_readfirstlane(__hip_atomic_load(c, __ATOMIC_RELAXED, __HIP_MEMORY_SCOPE_WORKGROUP));
    if (v >= want) break;
    __builtin_amdgcn_s_sleep(1);
  }
  CFENCE();
}

__device__ __forceinline__ void pace(u32 t) {
  asm volatile("1:\n\ts_memrealtime vcc\n\ts_waitcnt lgkmcnt(0)\n\ts_sub_u32 vcc_lo, vcc_lo, %0\n\ts_cmp_lt_i32 vcc_lo, 0\n\ts_cbranch_scc1 1b"
               :: "s"(t) : "vcc", "scc");
}
__device__ __forceinline__ void pace_tb(u32& vt, u32 tb) {
  asm volatile("1:\n\ts_memrealtime vcc\n\ts_waitcnt lgkmcnt(0)\n\ts_sub_u32 vcc_lo, vcc_lo, %1\n\ts_lshl_b32 vcc_lo, vcc_lo, 4\n\t"
               "s_cmp_lt_i32 vcc_lo, %0\n\ts_cbranch_scc1 1b\n\ts_sub_i32 vcc_lo, vcc_lo, %2\n\ts_max_i32 %0, %0, vcc_lo\n\ts_add_i32 %0, %0, %3"
               : "+s"(vt) : "s"(tb), "i"(BUCKET), "i"(PACE16) : "vcc", "scc");
}
__device__ __forceinline__ void pace_sleep(u32 t) {
  asm volatile("1:\n\ts_sleep 4\n\ts_memrealtime vcc\n\ts_waitcnt lgkmcnt(0)\n\ts_sub_u32 vcc_lo, vcc_lo, %0\n\ts_cmp_lt_i32 vcc_lo, 0\n\ts_cbranch_scc1 1b"
               :: "s"(t) : "vcc", "scc");
}

__global__ void __launch_bounds__(512, 2)
fused_decode(const void* a_x, const void* a_kc, const void* a_vc, const void* a_wq, const void* a_wo, const void* a_wg,
             const void* a_wu, const void* a_wd, void* a_out, void* a_ws) {
  v16u ka; v4u kb; u64 tb64;
  asm volatile("s_load_dwordx16 %0, %2, 0x0\n\ts_load_dwordx4 %1, %2, 0x40"
               : "=&s"(ka), "=&s"(kb) : "s"(__builtin_amdgcn_kernarg_segment_ptr()));
  __shared__ __attribute__((aligned(16))) u32 lds_[L_END];
  LAS u32* lds = (LAS u32*)lds_;
  const int c = blockIdx.x;
  const int h = (c & 7) * 4 + (c >> 6), j = (c >> 3) & 7, b = j >> 2, s = j & 3;
  const int id = h * 8 + j;
  const int lane = threadIdx.x & 63, wv = __builtin_amdgcn_readfirstlane(threadIdx.x >> 6);
#define KARG(v, i) ((char*)(((u64)(v)[2 * (i) + 1] << 32) | (v)[2 * (i)]))
#define STAMP_INIT() do {} while (0)

  u32 cpf;
  {
    u64 pc; u32 len;
    asm volatile("s_getpc_b64 %0\nfd_code_here:\n\ts_mov_b32 %1, fd_code_end-fd_code_here" : "=s"(pc), "=s"(len));
    cpf = __builtin_amdgcn_raw_buffer_load_b32(RSRC((char*)pc, len), lane << 20, (c >> 3) * 512 + wv * 64 + 64, 0);
    SB();
  }
  const int kvoff = ((b * 32 + h) * SK + (SK - WIN) + s * 512) * 256 + wv * 4096;
  const int lin = lane * 16;
  lds[lane & 15] = 0u;
  SB();
  u64 tptr;
  asm volatile("s_load_dwordx2 %0, %1, %2" : "=&s"(tptr) : "s"(__builtin_amdgcn_kernarg_segment_ptr()), "s"(((0x06538742u >> (4 * wv)) & 15u) * 8u));
  const int toffs = wv == 0 ? kvoff : wv == 1 ? h * 131072 : (wv & 3) == 3 ? 0 : (c >> 3) * (wv == 4 ? 131072 : 44032);
  asm volatile("s_waitcnt lgkmcnt(0)\n\ts_memrealtime %2\n\ts_barrier" : "+s"(ka), "+s"(kb), "=&s"(tb64), "+s"(tptr) : "s"(kvoff), "v"(lin), "s"(toffs) : "memory");
  SB();
  const auto rk = RSRC(KARG(ka, 1), 2 * 32 * SK * 256);
  v4u K[32];
  if (wv < 4) {
#pragma unroll
    for (int i = 0; i < BURST; ++i) { K[i] = LD16S(rk, lin + (i & 3) * 1024, kvoff + (i >> 2) * 16384, AUX_NT); SB(); }
  }
  asm volatile("s_waitcnt lgkmcnt(0)" : "+s"(tb64) :: "memory");
  const u32 tb = (u32)tb64 - KA;
  if (wv >= 4) asm volatile("s_load_dword s98, %0, %1" :: "s"(tptr), "s"(toffs) : "s98");

  if (wv < 4) {
    const int sw = wv;
    const auto rv = RSRC(KARG(ka, 2), 2 * 32 * SK * 256);
    v4u V[32];
    u32 vt = (BURST + LEAD) * PACE16;
#define THROTTLE(n) do { SB(); if (WINL) asm volatile("s_waitcnt vmcnt(%0)" :: "i"(WINL ? WINL - 1 : 0)); \
      if (BUCKET) pace_tb(vt, tb); else if (PACE16) pace(tb + ((((n) + LEAD) * PACE16) >> 4)); SB(); } while (0)
#define K_ISSUE(i) do { if ((i) >= BURST) THROTTLE(i); K[i] = LD16S(rk, lin + ((i) & 3) * 1024, kvoff + ((i) >> 2) * 16384, AUX_NT); SB(); } while (0)
#define V_ISSUE(i) do { THROTTLE(32 + (i)); V[i] = LD16S(rv, lin + ((i) & 3) * 1024, kvoff + ((i) >> 2) * 16384, AUX_NT); SB(); } while (0)
    STAMP_AT(6);
#pragma unroll
    for (int i = BURST; i < 32; ++i) K_ISSUE(i);
#pragma unroll
    for (int i = 0; i < 8; ++i) V_ISSUE(i);
    STAMP_AT(1);
    lds_wait(lds + C_Q, 1);
    asm volatile("" :: "v"(cpf));
    STAMP_AT(2);
    const int cc = lane & 15;
    const v4u q = *(const LAS v4u*)&lds[L_Q + cc * 4];
    float x[2][16];
#pragma unroll
    for (int i = 0; i < 32; ++i) {
      float d = dot2(K[i][0], q[0], 0.f);
      d = dot2(K[i][1], q[1], d); d = dot2(K[i][2], q[2], d); d = dot2(K[i][3], q[3], d);
      x[i & 1][i >> 1] = d;
      if ((i & 7) == 7) V_ISSUE(8 + (i >> 3));
    }
    float sc[2];
#define TR_STEP(HALF, CTRL) _Pragma("unroll") for (int t = 0; t < (HALF); ++t) { \
      const float t1 = x[g][t] + dppf<CTRL>(x[g][t]), t2 = x[g][t + (HALF)] + dppf<CTRL>(x[g][t + (HALF)]); x[g][t] = (cc & (HALF)) ? t2 : t1; }
#pragma unroll
    for (int g = 0; g < 2; ++g) {
      TR_STEP(8, XOR8) TR_STEP(4, XOR7) TR_STEP(2, XOR2) TR_STEP(1, XOR1)
      sc[g] = rbf(rbf(x[g][0]) * 0.08838834764831845f);
      V_ISSUE(12 + g);
    }
#undef TR_STEP
    const float m = wave_max(fmaxf(sc[0], sc[1]));
    V_ISSUE(14);
    const float p0 = __builtin_amdgcn_exp2f((sc[0] - m) * LOG2E), p1 = __builtin_amdgcn_exp2f((sc[1] - m) * LOG2E);
    const float l = wave_sum(p0 + p1);
    const u32 pp = cvtpk(p0, p1);
    *(LAS v2f*)&lds[L_ML + sw * 2] = v2f{m, l};
    asm volatile("s_mov_b64 exec, 1\n\tds_add_u32 %0, %1\n\ts_mov_b64 exec, -1" :: "v"((u32)(size_t)(lds + C_ML)), "v"(1u) : "memory");
    V_ISSUE(15);
    STAMP_AT(3);
    float acc[8];
#pragma unroll
    for (int e = 0; e < 8; ++e) acc[e] = 0.f;
#define PV_STEP(i) do { const u32 pb = dppu<0x150 + (i)>(pp); \
      _Pragma("unroll") for (int e = 0; e < 4; ++e) { \
        acc[2 * e] = dot2(plo(V[2 * (i)][e], V[2 * (i) + 1][e]), pb, acc[2 * e]); \
        acc[2 * e + 1] = dot2(phi(V[2 * (i)][e], V[2 * (i) + 1][e]), pb, acc[2 * e + 1]); } SB(); } while (0)
    PV_STEP(0); PV_STEP(1); V_ISSUE(16); PV_STEP(2); PV_STEP(3); V_ISSUE(17); PV_STEP(4); V_ISSUE(18); V_ISSUE(19);
    PV_STEP(5); V_ISSUE(20); V_ISSUE(21); PV_STEP(6); V_ISSUE(22); V_ISSUE(23); PV_STEP(7); V_ISSUE(24); V_ISSUE(25);
    PV_STEP(8); V_ISSUE(26); V_ISSUE(27); PV_STEP(9); V_ISSUE(28); V_ISSUE(29); PV_STEP(10); V_ISSUE(30); V_ISSUE(31);
    STAMP_AT(4);
    WGBAR();
    const float f = bitsf(lds[L_F + sw]);
    SB();
    PV_STEP(11); PV_STEP(12); PV_STEP(13); PV_STEP(14); PV_STEP(15);
    PIN(acc[0]); PIN(acc[7]);
    STAMP_AT(5);
    float t[4];
#pragma unroll
    for (int e = 0; e < 4; ++e) t[e] = red32(acc[e], acc[e + 4]);
    const float u0 = red16(t[0], t[2]) * f, u1 = red16(t[1], t[3]) * f;
    const int R = lane >> 4;
    *(LAS v2f*)&lds[L_RED + sw * 128 + cc * 8 + (R >> 1) * 4 + (R & 1) * 2] = v2f{u0, u1};
    WGBAR();
    asm volatile("fd_code_end:");
    return;
  }

  const int w = wv - 4;
  const int r8 = lane >> 3, c8 = lane & 7;
  const int ft = (w == 2) ? c : 256 + c;
  const bool has_tile = (w < 2) || (ft < 344);
  const int rstride = w < 2 ? 8192 : 5504;
  const int soff0 = w < 2 ? (128 * (j >> 1) + 64 * w) * 8192 + h * 256 + (j & 1) * 128 : ((ft >> 1) & 3) * 64 * 5504 + (ft >> 3) * 128;
  const int nrec = has_tile ? (w < 2 ? 512 * 8192 : 256 * 5504) : 0;
  const int toff = r8 * 8 * rstride + c8 * 16;
  const int xrec = w < 2 ? 2048 : (w == 2 ? 16 : 0), xoff = (w & 1) * 1024 + lane * 16;
  const char *xp = KARG(ka, 0), *wq = KARG(ka, 3), *wo = KARG(ka, 4), *wg = KARG(ka, 5), *wu = KARG(ka, 6), *wd = KARG(ka, 7);
  char *outp = KARG(kb, 0), *ws = KARG(kb, 1);
  STAMP_INIT();
  const auto rt = RSRC(w < 2 ? wq : ((ft & 1) ? wu : wg), nrec);
  const v4u xa = LD16(RSRC(w < 2 ? xp : ws, xrec), xoff, AUX_SC1);
  v4u T[8];
#pragma unroll
  for (int i = 0; i < 8; ++i) { if (TPACE && i >= T0NOW) pace(tb + TL0 + ((i * TPACE) >> 4)); T[i] = LD16S(rt, toff, soff0 + i * rstride, AUX_NT); SB(); }
  const auto rws = RSRC(ws, WS_END);
  __builtin_amdgcn_s_setprio(3);
  const int ch0 = (172 * h) >> 5, nch = ((172 * (h + 1)) >> 5) - ch0;
  u32 tag;
  {
    if (w < 2) {
      float v[8];
#pragma unroll
      for (int e = 0; e < 4; ++e) { v[2 * e] = bflo(xa[e]); v[2 * e + 1] = bfhi(xa[e]); }
      const float sum = ((v[0] + v[1]) + (v[2] + v[3])) + ((v[4] + v[5]) + (v[6] + v[7]));
      const float mean = wave_sum(sum) * (1.0f / 512.0f);
      float sq = 0.f;
#pragma unroll
      for (int e = 0; e < 8; ++e) { const float d = v[e] - mean; sq = fmaf(d, d, sq); }
      const float var = wave_sum(sq) * (1.0f / 512.0f);
      const float mb = rbf(mean);
      const float den = rbf(__builtin_amdgcn_sqrtf(rbf(rbf(var) + 1e-5f)));
      const float inv = __builtin_amdgcn_rcpf(den);
      v4u xn;
#pragma unroll
      for (int e = 0; e < 4; ++e) {
        const u32 dd = cvtpk(v[2 * e] - mb, v[2 * e + 1] - mb);
        xn[e] = cvtpk(bflo(dd) * inv, bfhi(dd) * inv);
      }
      *(LAS v4u*)&lds[L_XN + w * 256 + lane * 4] = xn;
      lds_post(lds + C_PREP, lane);
    } else if (w == 2) {
      if (lane == 0) lds[L_TAG] = xa[0] == 0xffffffffu ? 1u : xa[0] + 1u;
      lds_post(lds + C_PREP, lane);
    }
    lds_wait(lds + C_PREP, 3);
    asm volatile("" :: "v"(cpf));
    tag = __builtin_amdgcn_readfirstlane(lds[L_TAG]);
    u32 cf[2][4];
    if (w < 2) {
#pragma unroll
      for (int bb = 0; bb < 2; ++bb) {
        const v4u d = *(const LAS v4u*)&lds[L_XN + bb * 256 + (16 * (j >> 1) + 8 * w + r8) * 4];
#pragma unroll
        for (int k = 0; k < 4; ++k) cf[bb][k] = d[k];
      }
    } else {
      const u32 sel = ((ft >> 3) * 8 + c8 >= 172) ? 0x07060302u : 0x05040100u;
#pragma unroll
      for (int bb = 0; bb < 2; ++bb) {
        const LAS v4u* p = (const LAS v4u*)&lds[L_XN + bb * 256 + ((ft >> 1) & 3) * 64 + r8 * 8];
        const v4u d0 = p[0], d1 = p[1];
        cf[bb][0] = __builtin_amdgcn_perm(d0[1], d0[0], sel); cf[bb][1] = __builtin_amdgcn_perm(d0[3], d0[2], sel);
        cf[bb][2] = __builtin_amdgcn_perm(d1[1], d1[0], sel); cf[bb][3] = __builtin_amdgcn_perm(d1[3], d1[2], sel);
      }
    }
    float acc[2][8];
#pragma unroll
    for (int k = 0; k < 4; ++k)
#pragma unroll
      for (int e = 0; e < 4; ++e) {
        const u32 lo = plo(T[2 * k][e], T[2 * k + 1][e]), hi = phi(T[2 * k][e], T[2 * k + 1][e]);
#pragma unroll
        for (int bb = 0; bb < 2; ++bb) {
          acc[bb][2 * e] = dot2(lo, cf[bb][k], k ? acc[bb][2 * e] : 0.f);
          acc[bb][2 * e + 1] = dot2(hi, cf[bb][k], k ? acc[bb][2 * e + 1] : 0.f);
        }
      }
    float t[8];
#pragma unroll
    for (int e = 0; e < 8; ++e) t[e] = red32(acc[0][e], acc[1][e]);
    float u[4];
#pragma unroll
    for (int e = 0; e < 4; ++e) { u[e] = red16(t[e], t[e + 4]); u[e] += dppf<XOR8>(u[e]); }
    if (w < 2) {
      const bool hi8 = (lane & 8) != 0;
      float r0 = hi8 ? u[2] : u[0], r1 = hi8 ? u[3] : u[1];
      if (w == 1) {
        *(LAS v2f*)&lds[L_QP + lane * 2] = v2f{r0, r1};
        lds_post(lds + C_QP, lane);
      } else {
        lds_wait(lds + C_QP, 1);
        const v2f o = *(const LAS v2f*)&lds[L_QP + lane * 2];
        const int pos = (lane >> 5) * 64 + c8 * 8 + ((lane >> 4) & 1) * 4 + ((lane >> 3) & 1) * 2;
        ST16((v4u{fbits(r0 + o[0]), tag, fbits(r1 + o[1]), tag}), rws, WS_XQ + id * 1024 + pos * 8);
        const int goff = WS_XQ + (h * 8 + (lane >> 5)) * 1024 + b * 512 + (lane & 31) * 16;
        float s0 = 0.f, s1 = 0.f;
        __builtin_amdgcn_s_sleep(SLEEP0);
        pace_sleep(tb + QTIME);
#pragma nounroll
        for (u32 spin = 0; spin < SPIN_CAP; ++spin) {
          CFENCE();
          v4u g[4];
#pragma unroll
          for (int i = 0; i < 4; ++i) g[i] = LD16(rws, goff + i * 2048, AUX_SC1);
          u32 bad = 0;
#pragma unroll
          for (int i = 0; i < 4; ++i) bad |= (g[i][1] ^ tag) | (g[i][3] ^ tag);
          s0 = (bitsf(g[0][0]) + bitsf(g[1][0])) + (bitsf(g[2][0]) + bitsf(g[3][0]));
          s1 = (bitsf(g[0][2]) + bitsf(g[1][2])) + (bitsf(g[2][2]) + bitsf(g[3][2]));
          if (__all(bad == 0)) break;
          __builtin_amdgcn_s_sleep(8);
        }
        lds[L_Q + lane] = cvtpk(s0, s1);
        lds_post(lds + C_Q, lane);
      }
    } else {
      if (has_tile) {
        if (!(lane & 8)) ST16((v4u{fbits(u[0]), fbits(u[1]), fbits(u[2]), fbits(u[3])}), rws, WS_XF + ft * 512 + c8 * 64 + (lane >> 4) * 16);
        asm volatile("s_waitcnt vmcnt(0)" ::: "memory");
        if (lane == 0) __builtin_amdgcn_raw_buffer_store_b32(tag, rws, WS_FLAG + (ft >> 3) * 128 + (ft & 7) * 4, 0, AUX_SC1);
      }
      if (w == 3 && c >= 84) {
        const int uc = 255 - c, uu = uc + 172 * (lane >> 5), f = (uu >> 3) * 8 + ((lane >> 2) & 7);
        pace_sleep(tb + FTIME);
#pragma nounroll
        for (u32 spin = 0; spin < SPIN_CAP; ++spin) {
          CFENCE();
          const u32 fl = __builtin_amdgcn_raw_buffer_load_b32(rws, WS_FLAG + (f >> 3) * 128 + (f & 7) * 4, 0, AUX_SC1);
          if (__all(fl == tag)) break;
          __builtin_amdgcn_s_sleep(8);
        }
        CFENCE();
        const v4u g = LD16(rws, WS_XF + f * 512 + (uu & 7) * 64 + (lane & 3) * 16, AUX_SC1);
        float hv[4];
#pragma unroll
        for (int e = 0; e < 4; ++e) {
          float z = bitsf(g[e]);
          z += dppf<XOR8>(z); z = red16(z, z); z = red32(z, z);
          hv[e] = rbf(silu_f(rbf(z))) * rbf(dppf<ROR4>(z));
        }
        if (lane < 4) ST16((v4u{cvtpk(hv[0], hv[1]), tag, cvtpk(hv[2], hv[3]), tag}), rws, WS_XH + uc * 64 + lane * 16);
      }
    }
  }
  STAMP_AT(2);
  __builtin_amdgcn_s_setprio(0);
  const int oidx = (c >> 7) * 512 + 64 * j + ((c >> 6) & 1) * 32 + w * 8 + c8;
  u32 xres = __builtin_amdgcn_raw_buffer_load_b16(RSRC(xp, 2048), oidx * 2, 0, 0);
  asm volatile("" : "+v"(xres));
  if (w == 2) {
    pace_sleep(tb + HTIME);
    const bool v = (lane >> 2) < nch;
    const int hoff = v ? WS_XH + ch0 * 64 + lane * 16 : OOB;
    const u32 etag = v ? tag : 0u;
    v4u hg;
#pragma nounroll
    for (u32 spin = 0; spin < SPIN_CAP; ++spin) {
      CFENCE();
      hg = LD16(rws, hoff, AUX_SC1);
      if (__all(((hg[1] ^ etag) | (hg[3] ^ etag)) == 0)) break;
    }
    if (lane < 32) *(LAS v2u*)&lds[L_HB + lane * 2] = v2u{hg[0], hg[2]};
  }
  if (w == 1) {
    pace_sleep(tb + 770);
    lds_wait(lds + C_ML, 4);
    const v2u me = *(const LAS v2u*)&lds[L_ML + (lane & 3) * 2];
    ST16((v4u{me[0], tag, me[1], tag}), rws, lane < 4 ? WS_ML + id * 128 + lane * 16 : OOB);
    __builtin_amdgcn_s_sleep(SLEEP0);
    pace_sleep(tb + MTIME);
    const int moff = WS_ML + (h * 8 + b * 4 + ((lane >> 2) & 3)) * 128 + (lane & 3) * 16;
    v4u g;
#pragma nounroll
    for (u32 spin = 0; spin < SPIN_CAP; ++spin) {
      CFENCE();
      g = LD16(rws, lane < 16 ? moff : OOB, AUX_SC1);
      if (__all(lane >= 16 || ((g[1] ^ tag) | (g[3] ^ tag)) == 0)) break;
      __builtin_amdgcn_s_sleep(4);
    }
    const float mi = bitsf(g[0]);
    float M = fmaxf(mi, dppf<XOR1>(mi)); M = fmaxf(M, dppf<XOR2>(M)); M = fmaxf(M, dppf<XOR7>(M)); M = fmaxf(M, dppf<XOR8>(M));
    const float e = __builtin_amdgcn_exp2f((mi - M) * LOG2E);
    float L = e * bitsf(g[2]);
    L += dppf<XOR1>(L); L += dppf<XOR2>(L); L += dppf<XOR7>(L); L += dppf<XOR8>(L);
    if ((lane >> 2) == s) lds[L_F + (lane & 3)] = fbits(e * __builtin_amdgcn_rcpf(L));
  }
  const int g4 = lane >> 4;
  int a_red = L_RED + g4 * 128 + w * 32 + c8 * 4;
  int a_x1st = lane < 8 ? WS_X1 + id * 512 + w * 128 + lane * 16 : OOB;
  int a_x1ld = WS_X1 + (h * 8 + 2 * w) * 512 + lane * 16;
  int a_aw = L_A + (2 * w + (lane >> 5)) * 68 + (lane & 31) * 2;
  int a_ar = L_A + (lane & 7) * 68 + ((lane >> 3) & 1) * 32 + g4 * 4;
  int a_y = lane < 32 ? WS_X2 + id * 2048 + (g4 * 64 + w * 16 + (lane & 15)) * 16 : OOB;
  int a_g = WS_X2 + (r8 * 8 + j) * 2048 + ((c >> 6) * 32 + w * 8 + c8) * 16;
  int a_o = lane < 8 ? oidx * 2 : OOB;
  asm volatile("" : "+v"(a_red), "+v"(a_x1st), "+v"(a_x1ld), "+v"(a_aw), "+v"(a_ar), "+v"(a_y), "+v"(a_g), "+v"(a_o));
  bool win;
  STAMP_AT(1);
  WGBAR();
  STAMP_AT(3);
  {
    const auto ro = RSRC((const char*)wo + (size_t)h * 131072, 131072);
    const auto rd = RSRC(wd, 1376 * 1024);
    u32 old = 0;
    if (w == 3 && lane == 0)
      old = __hip_atomic_exchange((__attribute__((address_space(1))) u32*)(ws + WS_CLM + (c >> 3) * 128), tag, __ATOMIC_RELAXED, __HIP_MEMORY_SCOPE_AGENT);
    const u32 tv = (u32)__builtin_amdgcn_s_memrealtime();
    v4u U[6];
#pragma unroll
    for (int i = 0; i < 4; ++i) {
      if (WPACE && i) pace(WREL ? tv + ((i * WPACE) >> 4) - 8 : tb + (((63 + LEAD) * PACE16 + i * WPACE) >> 4));
      U[i] = LD16S(ro, (32 * w + r8) * 1024 + j * 128 + c8 * 16, i * 8192, AUX_NT);
      SB();
    }
#pragma unroll
    for (int i = 0; i < 2; ++i) U[4 + i] = LD16(rd, (2 * w + i < nch) ? (8 * (ch0 + 2 * w + i) + r8) * 1024 + j * 128 + c8 * 16 : OOB, AUX_NT);
    SB();
    const v4u ha0 = *(const LAS v4u*)&lds[L_HB + g4 * 8 + ((lane >> 2) & 1) * 4], ha1 = *(const LAS v4u*)&lds[L_HB + 32 + g4 * 8 + ((lane >> 2) & 1) * 4];
    WGBAR();
    {
      const v4f a = *(const LAS v4f*)&lds[a_red];
      const float e0 = red16(a[0], a[1]), e1 = red16(a[2], a[3]);
      const float f0 = red32(e0, e0), f1 = red32(e1, e1);
      auto p0 = __builtin_amdgcn_permlane16_swap(fbits(f0), fbits(f0), false, false);
      auto p1 = __builtin_amdgcn_permlane16_swap(fbits(f1), fbits(f1), false, false);
      ST16((v4u{cvtpk(bitsf((u32)p0[0]), bitsf((u32)p0[1])), tag, cvtpk(bitsf((u32)p1[0]), bitsf((u32)p1[1])), tag}), rws, a_x1st);
      SB();
      if (w == 0) STAMP_AT(9);
    }
    __builtin_amdgcn_s_sleep(SLEEP2);
    v4u g = LD16(rws, a_x1ld, AUX_SC1);
    SB();
    if (w != 0) STAMP_AT(9);
#pragma unroll
    for (int i = 0; i < 4; ++i) *(LAS v4u*)&lds[L_W + (32 * w + 8 * i + r8) * 36 + c8 * 4] = U[i];
#pragma unroll
    for (int i = 0; i < 2; ++i) *(LAS v4u*)&lds[L_W + (128 + 16 * w + 8 * i + r8) * 36 + c8 * 4] = U[4 + i];
    if (w == 3 && lane == 0) lds[L_WIN] = old;
    if (w != 3) STAMP_AT(8);
    WGBAR();
    win = __builtin_amdgcn_readfirstlane(lds[L_WIN]) != tag;
    v4u Bf[6];
    const int qq = (lane & 15) >> 2, pq = lane & 3;
    LAS u32* tp = lds + L_W + (8 * g4 + qq) * 36 + w * 8 + pq * 2;
#pragma unroll
    for (int t = 0; t < 6; ++t) {
      const v2u lo = __builtin_bit_cast(v2u, __builtin_amdgcn_ds_read_tr16_b64_v4i16((LAS v4s*)(tp + (32 * t) * 36)));
      const v2u hi = __builtin_bit_cast(v2u, __builtin_amdgcn_ds_read_tr16_b64_v4i16((LAS v4s*)(tp + (32 * t + 4) * 36)));
      Bf[t] = v4u{lo[0], lo[1], hi[0], hi[1]};
    }
    v4f dd = mfma16(ha0, Bf[4], v4f{0.f, 0.f, 0.f, 0.f});
    dd = mfma16(ha1, Bf[5], dd);
    float dsum = dd[0];
    asm volatile("" : "+v"(Bf[0]), "+v"(Bf[1]), "+v"(Bf[2]), "+v"(Bf[3]), "+v"(dsum));
    STAMP_AT(4);
#pragma nounroll
    for (u32 spin = 0; spin < SPIN_CAP; ++spin) {
      if (__all(((g[1] ^ tag) | (g[3] ^ tag)) == 0)) break;
      CFENCE();
      g = LD16(rws, a_x1ld, AUX_SC1);
    }
    *(LAS v2u*)&lds[a_aw] = v2u{g[0], g[2]};
    STAMP_AT(5);
    WGBAR();
    const LAS v4u* ap = (const LAS v4u*)&lds[a_ar];
    const v4u ax = ap[0], ay = ap[4];
    v4f d1 = mfma16(ax, Bf[0], v4f{0.f, 0.f, 0.f, 0.f}), d2 = mfma16(ax, Bf[2], v4f{0.f, 0.f, 0.f, 0.f});
    d1 = mfma16(ay, Bf[1], d1); d2 = mfma16(ay, Bf[3], d2);
    const float y1 = (d1[0] + d1[1]) + (d1[2] + d1[3]), y2 = (d2[0] + d2[1]) + (d2[2] + d2[3]);
    auto sw2 = __builtin_amdgcn_permlane32_swap(fbits(y2), fbits(y1), false, false);
    const float y = y1 + bitsf((u32)sw2[1]);
    ST16((v4u{fbits(y), tag, fbits(dsum), tag}), rws, a_y);
  }
  STAMP_AT(6);
  if (win) {
    __builtin_amdgcn_s_sleep(SLEEP4);
    v4u g[4];
#pragma nounroll
    for (u32 spin = 0; spin < SPIN_CAP; ++spin) {
      CFENCE();
#pragma unroll
      for (int i = 0; i < 4; ++i) g[i] = LD16S(rws, a_g, i * 64 * 2048, AUX_SC1);
      u32 bad = 0;
#pragma unroll
      for (int i = 0; i < 4; ++i) bad |= (g[i][1] ^ tag) | (g[i][3] ^ tag);
      if (__all(bad == 0)) break;
    }
    float ys = (bitsf(g[0][0]) + bitsf(g[1][0])) + (bitsf(g[2][0]) + bitsf(g[3][0]));
    float ds = (bitsf(g[0][2]) + bitsf(g[1][2])) + (bitsf(g[2][2]) + bitsf(g[3][2]));
    if (w == 3) STAMP_AT(8);
    ys += dppf<XOR8>(ys); ds += dppf<XOR8>(ds);
    float z = red16(ys, ds);
    z = red32(z, z);
    auto yd = __builtin_amdgcn_permlane16_swap(fbits(z), fbits(z), false, false);
    const float gt = rbf(silu_f(rbf(bitsf((u32)yd[1]))));
    const float res = bflo(xres) + rbf(rbf(bitsf((u32)yd[0])) * gt);
    __builtin_amdgcn_raw_buffer_store_b16((unsigned short)(cvtpk(res, res) & 0xffffu), RSRC(outp, 2048), a_o, 0, 0);
    if (c < 8 && w == 0 && lane == 0) __builtin_amdgcn_raw_buffer_store_b32(tag, rws, WS_EPOCH, 0, AUX_SC1);
    STAMP_AT(7);
  }
  STAMP_OUT();
}

extern "C" void kernel_launch(void* const* d_in, const int* in_sizes, int n_in, void* d_out, int out_size, void* d_ws,
                              size_t ws_size, hipStream_t stream) {
  fused_decode<<<256, 512, 0, stream>>>(d_in[0], d_in[1], d_in[2], d_in[3], d_in[4], d_in[6], d_in[7], d_in[8], d_out, d_ws);
}
```
